```python
import torch

def _softmax(x, axis=-1):
    return torch.softmax(x, dim=axis)

def _gelu(x):
    return torch.nn.functional.gelu(x, approximate='tanh')

def _silu(x):
    return torch.nn.functional.silu(x)

def _sigmoid(x):
    return torch.sigmoid(x)

def _layernorm(x, axis=-1, eps=1e-05):
    m = x.mean(dim=axis, keepdim=True)
    v = x.var(dim=axis, keepdim=True, unbiased=False)
    return (x - m) / torch.sqrt(v + eps)

def _scatter_add(tgt, idx, src, axis=0):
    return tgt.clone().index_add_(axis, idx, src)

def _segment_sum(src, seg, n):
    out = torch.zeros((n,) + src.shape[1:], dtype=src.dtype)
    return out.index_add_(0, seg, src)

def _pad(x, pw):
    flat = tuple((v for pair in reversed(pw) for v in pair))
    return torch.nn.functional.pad(x, flat)
kernel_name = 'standalone__ragged_sequences_varlen_flash_attn__ragged'

def setup_inputs():

    _w = lambda a: (lambda t: t.to(torch.bfloat16) if t.is_floating_point() else t)(torch.as_tensor(a))
    cu = torch.tensor([0, 899, 1287, 3063, 3381, 3962, 4737, 5351, 7180], dtype=torch.int64)
    T = int(cu[-1])
    Hq, Hkv, D = (16, 4, 128)
    assert Hq % Hkv == 0
    return {'q': _w((lambda *s: torch.randn(*s, dtype=torch.bfloat16) * 0.8)(T, Hq, D)), 'k': _w((lambda *s: torch.randn(*s, dtype=torch.bfloat16) * 0.8)(T, Hkv, D)), 'v': _w((lambda *s: torch.randn(*s, dtype=torch.bfloat16) * 0.05)(T, Hkv, D)), 'cu': _w(cu)}

def reference(q, k, v, cu):
    T, Hq, D = q.shape
    Hkv = k.shape[1]
    rep = Hq // Hkv
    scale = D ** (-0.5)
    outs = []
    for i in range(len(cu) - 1):
        s, e = (int(cu[i]), int(cu[i + 1]))
        L = e - s
        qi = q[s:e].clone()
        ki = (lambda x, n, axis: torch.repeat_interleave(x, n, dim=axis))(k[s:e], rep, axis=1)
        vi = (lambda x, n, axis: torch.repeat_interleave(x, n, dim=axis))(v[s:e], rep, axis=1)
        logits = torch.einsum('qhd,khd->hqk', qi, ki) * scale
        ar = torch.arange(L)
        neg = (lambda x, dt: x.to(dt))(ar[None, :] > ar[:, None], logits.dtype) * -1000000000.0
        p = _softmax(logits + neg[None, :, :], axis=-1)
        outs.append(torch.einsum('hqk,khd->qhd', p, vi))
    return torch.concatenate(outs, axis=0)
```

```cpp
#include <hip/hip_runtime.h>
#include <hip/hip_bf16.h>
#include <stdint.h>

#define NBIN 64
#define WSLOT 327680u
#define DL 1u
#define SG 2
#define FL_DIAG (1u << 16)
#define FL_POUT (1u << 17)
#define FL_PIN (1u << 18)

static __device__ __forceinline__ int rl(int v, int i) { return __builtin_amdgcn_readlane(v, i); }

__global__ __launch_bounds__(512, 2) void varlen_attn_kernel(const char* __restrict__ q, const char* __restrict__ k,
                                                             const char* __restrict__ v, const int* __restrict__ cu,
                                                             char* __restrict__ o, char* __restrict__ ws, int nseq, int T,
                                                             unsigned wbytes, int chunk) {
  __shared__ __attribute__((aligned(1024))) char lds[163840];
  const unsigned tid = threadIdx.x, l = tid & 63u, c = l & 15u, g = l >> 4;
  const unsigned w = __builtin_amdgcn_readfirstlane(tid >> 6);
  unsigned cpf;
  {
    const char* pc; unsigned off;
    asm volatile("s_getpc_b64 %1\n.Lattn_pc:\n v_min_u32 %2, .Lattn_end-.Lattn_pc-4, %3\n global_load_dword %0, %2, %1"
                 : "=v"(cpf), "=&s"(pc), "=&v"(off) : "v"(tid * 64u));
  }
  if (w < 4) __builtin_amdgcn_s_setprio(1);
  const int kvh = (blockIdx.x & 7) >> 1, bin = (int)((blockIdx.x >> 3) * 2 + (blockIdx.x & 1));
  int s0, s1;
  asm volatile("global_load_dword %0, %2, %4\n global_load_dword %1, %3, %4"
               : "=&v"(s0), "=&v"(s1) : "v"(4u * min(l, (unsigned)nseq)), "v"(4u * min(l + 1u, (unsigned)nseq)), "s"(cu));

  const unsigned ldsb = (unsigned)(size_t)lds;
  const unsigned cg = 2u * (g & 1u) + (g >> 1);
  unsigned kb0 = ldsb + 128u * c + 16u * ((0u + cg) ^ ((c >> 1) & 7u));
  const unsigned vbb = ldsb + 16384u + 2048u * (g >> 1) + 64u * (4u * (g & 1u) + ((l >> 2) & 3u)) + 8u * (l & 3u);
  unsigned vb0 = vbb + 32u * (g & 1u);
  const unsigned krow = 8u * w + (l >> 3);
  unsigned ksrc = krow * 1024u + 16u * ((l & 7u) ^ ((krow >> 1) & 7u));
  unsigned vsrc = (8u * w + ((l >> 2) & 7u)) * 1024u + 64u * (l >> 5) + 16u * ((l & 3u) ^ (2u * ((l >> 4) & 1u)));
  unsigned qs = w * 32768u + g * 256u + 64u * (c >> 2) + 16u * ((l & 3u) ^ g);
  unsigned la = ldsb + 131072u + w * 4096u + 16u * l;
  unsigned sa = ldsb + 131072u + w * 4096u + c * 256u + 16u * (cg ^ (c & 3u));
  unsigned qpf = (8u * w + (l >> 5)) * 4096u + (l & 31u) * 32u;
  int mt = (int)(8u * w + (c >> 2)) - 4 * (int)g;
  unsigned woff = 262144u + kvh * 15360u + w * 1024u + 16u * l;
  const char* pk = k + kvh * 256;
  const char* pv = v + kvh * 256;
  const char* pq = q + kvh * 1024;
  char* po = o + kvh * 1024;
  char* pw = ws + kvh * 1024;
  unsigned qbytes = (unsigned)(T - 1) * 4096u + 1024u;
  const unsigned lp = max(l, DL + 1u) - DL;
  int ih = 4 * (int)(lp >> 1), imh = 4 * (int)min(l + (lp >> 1), 63u), im1 = 4 * (int)max(l, 1u) - 4, im2 = 4 * (int)lp - 4;
  int cap = l < 2 + DL || nseq > 64 ? -1 : l < 8 ? 0 : (int)(wbytes / WSLOT), lsum = (int)min(l + lp, 63u) - 1, lim = (int)lp * NBIN;
  asm volatile("" : "+v"(cap), "+v"(lsum), "+v"(lim));
  asm volatile("" : "+v"(kb0), "+v"(vb0), "+v"(ksrc), "+v"(vsrc), "+v"(qs), "+v"(la), "+v"(sa), "+v"(qpf),
                    "+v"(mt), "+v"(woff), "+v"(ih), "+v"(imh), "+v"(im1), "+v"(im2), "+s"(pk), "+s"(pv), "+s"(pq), "+s"(po), "+s"(pw), "+s"(qbytes));
  asm volatile("s_waitcnt vmcnt(0)" : "+v"(s0), "+v"(s1) : "v"(cpf));
  const int n = (s1 - s0 + 63) >> 6;

  unsigned suf = 0, sq = 0;
#pragma unroll
  for (int u = 0; u < 8; ++u) {
    const unsigned a = (unsigned)rl(n, u);
    suf += __builtin_elementwise_sub_sat(a, l);
    sq += a * a;
  }
  for (int u = 8; u < min(nseq, 64); ++u) {
    const unsigned a = (unsigned)rl(n, u);
    suf += __builtin_elementwise_sub_sat(a, l);
    sq += a * a;
  }
  const int vfull = __builtin_amdgcn_ds_bpermute(im2, (int)suf), sm1 = __builtin_amdgcn_ds_bpermute(im1, (int)suf);
  const int sufh = __builtin_amdgcn_ds_bpermute(ih, (int)suf), sufmh = __builtin_amdgcn_ds_bpermute(imh, (int)suf);
  const int nmax = __builtin_popcountll(__ballot(suf != 0)), nit = rl((int)suf, 0);
  const int total = ((int)sq + nit) >> 1;
  const int base = sm1 - (int)suf, vfp = sufh + sufmh;
  const int vbl = NBIN - vfp, vcnt = nit + (int)suf + vfull - 2 * vfp;
  const int bad = max(max(max(nmax - lsum, (int)suf - cap), -vbl),
                      min(vcnt, max(total - (DL ? sm1 : 0) + SG * vcnt + (1 - 2 * SG) * vbl - lim, vcnt - 62 * vbl)));
  const unsigned long long okm = __ballot(bad <= 0);
  const bool fast = okm != 0;
  if (fast && chunk > 0) return;
  {
    unsigned ta = 0x7f000000u, tb = 0, tc = 0, td = 0, tn = 0x7fffffffu;
    if (fast) {
      const int M = __builtin_ctzll(okm), P = M - (int)DL;
      const int nfull = rl(vfull, M), nfp = rl(vfp, M);
      int e0 = 0, e1 = 0, ez = 0, ef = 0, en = 0x7fffffff, t0 = 0;
      auto emit = [&](int idx, int z, int i, unsigned fl, int endstep) {
        const unsigned long long m = __ballot(n >= z);
        const int r = __builtin_amdgcn_mbcnt_hi((unsigned)(m >> 32), __builtin_amdgcn_mbcnt_lo((unsigned)m, 0u));
        const int s = __builtin_ctzll(__ballot(r == i) & m);
        asm("s_nop 3\n\tv_writelane_b32 %0, %5, m0\n\tv_writelane_b32 %1, %6, m0\n\tv_writelane_b32 %2, %7, m0\n\t"
            "v_writelane_b32 %3, %8, m0\n\tv_writelane_b32 %4, %9, m0"
            : "+v"(e0), "+v"(e1), "+v"(ez), "+v"(ef), "+v"(en)
            : "s"(rl(s0, s)), "s"(rl(s1, s)), "s"(z), "s"(fl), "s"(endstep), "{m0}"(idx));
      };
      auto emit_class = [&](int idx, int kc, int ic, int endstep) {
        const int zk = min(M + kc, 63), c2 = rl(base, zk), sk = rl((int)suf, zk);
        const bool prod = ic < c2;
        emit(idx, prod ? zk : kc, prod ? ic : ic - c2, prod ? (FL_POUT | 1u | (unsigned)(sk + ic) << 19) : FL_DIAG | 1u, endstep);
      };
      if (bin < nfull) {
        const int z = __builtin_ctzll(__ballot((int)suf <= bin)), nt = min(z, M);
        tb = z > M ? 1u : 0u; t0 = z - nt;
        emit(0, z, bin - rl((int)suf, z), FL_DIAG | 1u | (z > M ? (FL_PIN | (unsigned)bin << 19) : 0u), nt - 1);
      } else {
        const int sm = __builtin_amdgcn_ds_bpermute(4 * min((int)l + M, 63), (int)suf);
        const int cum = (int)suf - nfull + sm;
        if (bin < nfp) {
          const int r = bin - nfull;
          const int kc = __builtin_ctzll(__ballot(cum <= r));
          const int ic = r - rl(cum, kc);
          const int sw = ic < rl(base, min(M + P - kc, 63)) ? 1 : 0;
          emit_class(sw, kc, ic, sw ? P - 1 : kc - 1);
          emit_class(1 - sw, P - kc, ic, sw ? P - kc - 1 : P - 1);
        } else {
          const int h = P >> 1, b = bin - nfp, BL = NBIN - nfp, lcnt = rl(vcnt, M);
          const int cr = __builtin_amdgcn_ds_bpermute(4 * max(P - 1 - (int)l, h), cum);
          const int cs = __builtin_amdgcn_ds_bpermute(4 * max(P - (int)l, h), cum);
          const int lcum = cum + cr - 2 * (nfp - nfull);
          const int adj = cr - cs - lcum;
          int E = lcnt;
          while (E >= BL) E -= BL;
          const int Y = BL - E, gb = b >= Y ? b - Y : b, gn = b >= Y ? E : Y;
          int es = -1;
          for (int rd = 0;; ++rd) {
            const int r0 = rd * BL;
            const bool fullr = r0 + BL <= lcnt;
            if (r0 >= lcnt || (!fullr && b < Y)) break;
            const int r = r0 + ((__builtin_popcount(rd) & 1) ? gn - 1 - gb : gb) + (b >= Y && fullr ? Y : 0);
            const int kc = __builtin_ctzll(__ballot(lcum <= r));
            es += kc;
            emit_class(rd, kc, r + rl(adj, kc), es);
          }
        }
      }
      td = (unsigned)ef; tn = (unsigned)en; tc = (unsigned)(e1 - 1);
      ta = ef ? (unsigned)(e0 + 64 * (ez - 1)) * 4096u : ta;
      tb = ef ? (unsigned)(e0 + 64 * t0) * 1024u : tb;
    } else {
      const long long want = (long long)bin + (long long)NBIN * ((long long)chunk * 62 + l);
      long long pre = 0;
      for (int s = 0; s < nseq; ++s) {
        const int b0 = cu[s], b1 = cu[s + 1], a = (b1 - b0 + 63) >> 6;
        if (l < 62 && want >= pre && want < pre + a) {
          const int j = a - 1 - (int)(want - pre);
          ta = (unsigned)(b0 + 64 * j) * 4096u; tb = (unsigned)b0 * 1024u; tc = (unsigned)(b1 - 1);
          td = (unsigned)(j + 1) | FL_DIAG;
        }
        pre += a;
      }
      int es = -1;
      for (int i = 0; i < 62; ++i) {
        es += rl((int)td, i) & 0xffff;
        if ((int)l == i && td) tn = (unsigned)es;
      }
    }
    const unsigned slo = (td >> 19) * WSLOT;
    const unsigned epa = (td & (FL_POUT | FL_PIN)) ? slo + (woff & ~1023u) + 8320u : 0xfffffff0u;
    const unsigned tk = td ? tc * 1024u + 256u : 0u;
    td = (td & ~0xfffeu) | (td ? 1u : 0u) | ((td && ta + w * 32768u >= tk * 4u) ? 0x8000u : 0u);
    const unsigned te = (td & FL_POUT) ? wbytes : tk * 4u, tq = (td & FL_POUT) ? slo : ta;
    if (rl((int)td, 0) != 0) {
      asm volatile(
    "s_getpc_b64 s[56:57]\n"
    "BASE_%=:\n"
    "v_readlane_b32 s30, %[tk], 0\n"
    "v_readlane_b32 s67, %[tb], 0\n"
    "v_readlane_b32 s89, %[ta], 0\n"
    "v_readlane_b32 s66, %[tn], 0\n"
    "s_mov_b64 s[28:29], %[pk]\n"
    "s_mov_b32 s31, 0x00020000\n"
    "s_mov_b64 s[36:37], %[pq]\n"
    "s_mov_b32 s39, 0x00020000\n"
    "s_lshl_b32 s70, %[wave], 10\n"
    "s_add_u32 s70, s70, %[lds]\n"
    "s_mov_b32 s68, 0\n"
    "s_mov_b32 s90, 8064\n"
    "s_add_u32 m0, s68, s70\n"
    "s_nop 0\n"
    "buffer_load_dwordx4 %[ksrc], s[28:31], s67 offen lds\n"
    "s_add_u32 m0, m0, s90\n"
    "s_nop 0\n"
    "buffer_load_dwordx4 %[ksrc], s[28:31], s67 offen offset:128 lds\n"
    "s_mov_b64 s[32:33], %[pv]\n"
    "s_mov_b32 s35, 0x00020000\n"
    "s_mov_b64 s[44:45], %[pw]\n"
    "s_mov_b32 s47, 0x00020000\n"
    "s_mov_b32 s34, s30\n"
    "s_mov_b32 s38, %[qbytes]\n"
    "s_mov_b32 s46, %[wbytes]\n"
    "s_lshl_b32 s71, %[wave], 11\n"
    "s_add_u32 s71, s71, %[lds]\n"
    "s_add_u32 s71, s71, 16384\n"
    "s_mov_b32 s69, 0x1ffff\n"
    "s_lshl_b32 s75, %[wave], 13\n"
    "s_add_u32 s75, s75, %[lds]\n"
    "s_add_u32 s75, s75, 65536\n"
    "s_cmp_ge_u32 %[wave], 4\n"
    "s_cbranch_scc1 PRO_B_%=\n"
    "s_add_u32 m0, s75, 0\n"
    "s_add_u32 s72, s89, 0\n"
    "buffer_load_dwordx4 %[qs], s[36:39], s72 offen lds\n"
    "s_add_u32 m0, s75, 1024\n"
    "s_add_u32 s72, s89, 4096\n"
    "buffer_load_dwordx4 %[qs], s[36:39], s72 offen lds\n"
    "s_add_u32 m0, s75, 2048\n"
    "s_add_u32 s72, s89, 8192\n"
    "buffer_load_dwordx4 %[qs], s[36:39], s72 offen lds\n"
    "s_add_u32 m0, s75, 3072\n"
    "s_add_u32 s72, s89, 12288\n"
    "buffer_load_dwordx4 %[qs], s[36:39], s72 offen lds\n"
    "s_add_u32 m0, s75, 4096\n"
    "s_add_u32 s72, s89, 16384\n"
    "buffer_load_dwordx4 %[qs], s[36:39], s72 offen lds\n"
    "s_add_u32 m0, s75, 5120\n"
    "s_add_u32 s72, s89, 20480\n"
    "buffer_load_dwordx4 %[qs], s[36:39], s72 offen lds\n"
    "s_add_u32 m0, s75, 6144\n"
    "s_add_u32 s72, s89, 24576\n"
    "buffer_load_dwordx4 %[qs], s[36:39], s72 offen lds\n"
    "s_add_u32 m0, s75, 7168\n"
    "s_add_u32 s72, s89, 28672\n"
    "buffer_load_dwordx4 %[qs], s[36:39], s72 offen lds\n"
    "s_barrier\n"
    "s_branch PRO_1_%=\n"
    "PRO_B_%=:\n"
    "s_barrier\n"
    "s_add_u32 m0, s75, 0\n"
    "s_add_u32 s72, s89, 0\n"
    "buffer_load_dwordx4 %[qs], s[36:39], s72 offen lds\n"
    "s_add_u32 m0, s75, 1024\n"
    "s_add_u32 s72, s89, 4096\n"
    "buffer_load_dwordx4 %[qs], s[36:39], s72 offen lds\n"
    "s_add_u32 m0, s75, 2048\n"
    "s_add_u32 s72, s89, 8192\n"
    "buffer_load_dwordx4 %[qs], s[36:39], s72 offen lds\n"
    "s_add_u32 m0, s75, 3072\n"
    "s_add_u32 s72, s89, 12288\n"
    "buffer_load_dwordx4 %[qs], s[36:39], s72 offen lds\n"
    "s_add_u32 m0, s75, 4096\n"
    "s_add_u32 s72, s89, 16384\n"
    "buffer_load_dwordx4 %[qs], s[36:39], s72 offen lds\n"
    "s_add_u32 m0, s75, 5120\n"
    "s_add_u32 s72, s89, 20480\n"
    "buffer_load_dwordx4 %[qs], s[36:39], s72 offen lds\n"
    "s_add_u32 m0, s75, 6144\n"
    "s_add_u32 s72, s89, 24576\n"
    "buffer_load_dwordx4 %[qs], s[36:39], s72 offen lds\n"
    "s_add_u32 m0, s75, 7168\n"
    "s_add_u32 s72, s89, 28672\n"
    "buffer_load_dwordx4 %[qs], s[36:39], s72 offen lds\n"
    "PRO_1_%=:\n"
    "s_add_u32 m0, s68, s71\n"
    "s_add_u32 s68, s68, 32768\n"
    "buffer_load_dwordx4 %[vsrc], s[32:35], s67 offen lds\n"
    "s_add_u32 m0, m0, 896\n"
    "s_and_b32 s68, s68, s69\n"
    "buffer_load_dwordx4 %[vsrc], s[32:35], s67 offen offset:128 lds\n"
    "s_add_u32 s67, s67, 65536\n"
    "s_mov_b32 s43, 0x00020000\n"
    "s_mov_b32 s64, 0x3e0293ee\n"
    "s_mov_b32 s65, 0\n"
    "s_mov_b32 s76, -1\n"
    "s_mov_b32 s77, -1\n"
    "s_mov_b32 s78, -1\n"
    "s_mov_b32 s86, 0\n"
    "s_mov_b32 s87, -1\n"
    "s_mov_b32 s96, G_%=-BASE_%=\n"
    "s_lshl_b32 s82, %[wave], 12\n"
    "s_add_u32 s82, s82, %[lds]\n"
    "s_add_u32 s82, s82, 131072\n"
    "s_sub_u32 s75, s75, s82\n"
    "v_add_u32 v228, s75, %[sa]\n"
    "s_lshr_b32 s97, %[wave], 2\n"
    "s_add_u32 s97, s97, 2\n"
    "s_sub_u32 s92, 0, s97\n"
    "s_sub_u32 s66, s66, s97\n"
    "s_cmp_lg_u32 s92, s66\n"
    "s_cbranch_scc1 PA1_%=\n"
    "s_add_u32 s65, s65, 1\n"
    "v_readlane_b32 s30, %[tk], s65\n"
    "v_readlane_b32 s67, %[tb], s65\n"
    "v_readlane_b32 s66, %[tn], s65\n"
    "s_mov_b32 s34, s30\n"
    "s_sub_u32 s66, s66, s97\n"
    "s_cmp_eq_u32 s67, 1\n"
    "s_cselect_b32 s69, 0, s69\n"
    "s_cselect_b32 s68, 0, s68\n"
    "s_cselect_b32 s90, 896, s90\n"
    "s_cselect_b32 s70, s82, s70\n"
    "s_cselect_b32 s71, s82, s71\n"
    "PA1_%=:\n"
    "s_add_u32 m0, s68, s70\n"
    "s_nop 0\n"
    "buffer_load_dwordx4 %[ksrc], s[28:31], s67 offen lds\n"
    "s_add_u32 m0, m0, s90\n"
    "s_nop 0\n"
    "buffer_load_dwordx4 %[ksrc], s[28:31], s67 offen offset:128 lds\n"
    "s_add_u32 m0, s68, s71\n"
    "s_add_u32 s68, s68, 32768\n"
    "buffer_load_dwordx4 %[vsrc], s[32:35], s67 offen lds\n"
    "s_add_u32 m0, m0, 896\n"
    "s_and_b32 s68, s68, s69\n"
    "buffer_load_dwordx4 %[vsrc], s[32:35], s67 offen offset:128 lds\n"
    "s_add_u32 s67, s67, 65536\n"
    "s_add_u32 s92, s92, 1\n"
    "v_mbcnt_lo_u32_b32 v233, -1, 0\n"
    "v_mbcnt_hi_u32_b32 v233, -1, v233\n"
    "v_add_u32 v233, 3, v233\n"
    "v_and_b32 v230, 0xfc, v233\n"
    "v_cmp_gt_u32 vcc, 4, v233\n"
    "v_mov_b32 v231, 0xfc\n"
    "v_and_b32 v233, 3, v233\n"
    "v_cndmask_b32 v230, v230, v231, vcc\n"
    "v_writelane_b32 v230, -4, 63\n"
    "ds_bpermute_b32 v231, v230, %[ta]\n"
    "v_lshrrev_b32 v235, 4, %[la]\n"
    "v_and_b32 v235, 16, v235\n"
    "v_lshrrev_b32 v234, 1, v235\n"
    "v_lshlrev_b32 v235, 1, v235\n"
    "v_xor_b32 v235, v235, %[sa]\n"
    "v_add_u32 v234, v234, v235\n"
    "v_xor_b32 v235, 32, v234\n"
    "v_mov_b32 v222, %[kb0]\n"
    "v_xor_b32 v223, 64, %[kb0]\n"
    "v_mov_b32 v216, 0x3f803f80\n"
    "v_mov_b32 v217, 0x3f803f80\n"
    "v_mov_b32 v218, 0x3f803f80\n"
    "v_mov_b32 v219, 0x3f803f80\n"
    "s_lshr_b32 s72, %[wave], 1\n"
    "s_add_u32 s52, s56, MASK0_%=-BASE_%=\n"
    "s_addc_u32 s53, s57, 0\n"
    "s_cmp_ge_u32 s72, 1\n"
    "s_cselect_b32 s73, MASK1_%=-MASK0_%=, 0\n"
    "s_add_u32 s52, s52, s73\n"
    "s_addc_u32 s53, s53, 0\n"
    "s_cmp_ge_u32 s72, 2\n"
    "s_cselect_b32 s73, MASK2_%=-MASK1_%=, 0\n"
    "s_add_u32 s52, s52, s73\n"
    "s_addc_u32 s53, s53, 0\n"
    "s_cmp_ge_u32 s72, 3\n"
    "s_cselect_b32 s73, MASK3_%=-MASK2_%=, 0\n"
    "s_add_u32 s52, s52, s73\n"
    "s_addc_u32 s53, s53, 0\n"
    "s_waitcnt lgkmcnt(0)\n"
    "v_lshl_add_u32 v233, v233, 13, v231\n"
    "s_cmp_lt_u32 %[wave], 4\n"
    "s_cselect_b64 vcc, 0, -1\n"
    "s_cbranch_scc1 PRO_A_%=\n"
    "s_waitcnt vmcnt(14)\n"
    "s_barrier\n"
    "s_cmp_lg_u32 s92, s66\n"
    "s_cbranch_scc1 PA2_%=\n"
    "s_add_u32 s65, s65, 1\n"
    "v_readlane_b32 s30, %[tk], s65\n"
    "v_readlane_b32 s67, %[tb], s65\n"
    "v_readlane_b32 s66, %[tn], s65\n"
    "s_mov_b32 s34, s30\n"
    "s_sub_u32 s66, s66, s97\n"
    "s_cmp_eq_u32 s67, 1\n"
    "s_cselect_b32 s69, 0, s69\n"
    "s_cselect_b32 s68, 0, s68\n"
    "s_cselect_b32 s90, 896, s90\n"
    "s_cselect_b32 s70, s82, s70\n"
    "s_cselect_b32 s71, s82, s71\n"
    "PA2_%=:\n"
    "s_add_u32 m0, s68, s70\n"
    "s_nop 0\n"
    "buffer_load_dwordx4 %[ksrc], s[28:31], s67 offen lds\n"
    "s_add_u32 m0, m0, s90\n"
    "s_nop 0\n"
    "buffer_load_dwordx4 %[ksrc], s[28:31], s67 offen offset:128 lds\n"
    "s_add_u32 m0, s68, s71\n"
    "s_add_u32 s68, s68, 32768\n"
    "buffer_load_dwordx4 %[vsrc], s[32:35], s67 offen lds\n"
    "s_add_u32 m0, m0, 896\n"
    "s_and_b32 s68, s68, s69\n"
    "buffer_load_dwordx4 %[vsrc], s[32:35], s67 offen offset:128 lds\n"
    "s_add_u32 s67, s67, 65536\n"
    "s_add_u32 s92, s92, 1\n"
    "buffer_load_dword v226, %[epa], s[44:47], 0 offen sc1\n"
    "s_barrier\n"
    "s_waitcnt vmcnt(11)\n"
    "ds_read_b128 v[64:67], v228 offset:0\n"
    "ds_read_b128 v[68:71], v228 offset:64\n"
    "ds_read_b128 v[72:75], v228 offset:128\n"
    "ds_read_b128 v[76:79], v228 offset:192\n"
    "ds_read_b128 v[80:83], v228 offset:4096\n"
    "ds_read_b128 v[84:87], v228 offset:4160\n"
    "ds_read_b128 v[88:91], v228 offset:4224\n"
    "ds_read_b128 v[92:95], v228 offset:4288\n"
    "s_branch PRO_3_%=\n"
    "PRO_A_%=:\n"
    "buffer_load_dword v226, %[epa], s[44:47], 0 offen sc1\n"
    "s_waitcnt vmcnt(7)\n"
    "ds_read_b128 v[64:67], v228 offset:0\n"
    "ds_read_b128 v[68:71], v228 offset:64\n"
    "ds_read_b128 v[72:75], v228 offset:128\n"
    "ds_read_b128 v[76:79], v228 offset:192\n"
    "ds_read_b128 v[80:83], v228 offset:4096\n"
    "ds_read_b128 v[84:87], v228 offset:4160\n"
    "ds_read_b128 v[88:91], v228 offset:4224\n"
    "ds_read_b128 v[92:95], v228 offset:4288\n"
    "s_waitcnt lgkmcnt(0)\n"
    "s_barrier\n"
    "PRO_3_%=:\n"
    "ds_read_b128 v[144:147], v222 offset:0\n"
    "ds_read_b128 v[148:151], v223 offset:0\n"
    "ds_read_b128 v[152:155], v222 offset:8192\n"
    "ds_read_b128 v[156:159], v223 offset:8192\n"
    "ds_read_b128 v[160:163], v222 offset:2048\n"
    "ds_read_b128 v[164:167], v223 offset:2048\n"
    "ds_read_b128 v[168:171], v222 offset:10240\n"
    "s_cmp_lg_u32 s92, s66\n"
    "s_cbranch_scc1 PA3_%=\n"
    "s_add_u32 s65, s65, 1\n"
    "v_readlane_b32 s30, %[tk], s65\n"
    "v_readlane_b32 s67, %[tb], s65\n"
    "v_readlane_b32 s66, %[tn], s65\n"
    "s_mov_b32 s34, s30\n"
    "s_sub_u32 s66, s66, s97\n"
    "s_cmp_eq_u32 s67, 1\n"
    "s_cselect_b32 s69, 0, s69\n"
    "s_cselect_b32 s68, 0, s68\n"
    "s_cselect_b32 s90, 896, s90\n"
    "s_cselect_b32 s70, s82, s70\n"
    "s_cselect_b32 s71, s82, s71\n"
    "PA3_%=:\n"
    "s_setprio 1\n"
    "s_waitcnt lgkmcnt(5)\n"
    "v_mfma_f32_16x16x32_bf16 v[96:99], v[144:147], v[64:67], 0\n"
    "ds_read_b128 v[172:175], v223 offset:10240\n"
    "s_cmp_eq_u32 s92, s77\n"
    "v_mfma_f32_16x16x32_bf16 v[96:99], v[148:151], v[68:71], v[96:99]\n"
    "v_add_u32 v224, s86, %[vb0]\n"
    "s_cselect_b32 s85, 1, 0\n"
    "s_addc_u32 s76, s76, 0\n"
    "s_waitcnt lgkmcnt(4)\n"
    "v_mfma_f32_16x16x32_bf16 v[96:99], v[152:155], v[72:75], v[96:99]\n"
    "ds_read_b128 v[176:179], v222 offset:4096\n"
    "s_sub_u32 s72, s76, 1\n"
    "v_mfma_f32_16x16x32_bf16 v[96:99], v[156:159], v[76:79], v[96:99]\n"
    "v_xor_b32 v225, 32, v224\n"
    "s_add_u32 s74, s76, 1\n"
    "v_readlane_b32 s77, %[tn], s76\n"
    "v_mfma_f32_16x16x32_bf16 v[100:103], v[144:147], v[80:83], 0\n"
    "ds_read_b128 v[180:183], v223 offset:4096\n"
    "v_readlane_b32 s79, %[td], s76\n"
    "v_readlane_b32 s88, %[td], s74\n"
    "v_mfma_f32_16x16x32_bf16 v[100:103], v[148:151], v[84:87], v[100:103]\n"
    "v_readlane_b32 s89, %[ta], s74\n"
    "v_readlane_b32 s80, %[td], s72\n"
    "v_readlane_b32 s81, %[tq], s72\n"
    "v_mfma_f32_16x16x32_bf16 v[100:103], v[152:155], v[88:91], v[100:103]\n"
    "ds_read_b128 v[184:187], v222 offset:12288\n"
    "v_readlane_b32 s42, %[te], s72\n"
    "s_bitcmp1_b32 s80, 17\n"
    "v_mfma_f32_16x16x32_bf16 v[100:103], v[156:159], v[92:95], v[100:103]\n"
    "s_cselect_b64 s[40:41], s[44:45], %[po]\n"
    "s_cselect_b32 s83, s81, 0x7f000000\n"
    "s_cselect_b32 s75, s85, 0\n"
    "s_waitcnt lgkmcnt(5)\n"
    "v_mfma_f32_16x16x32_bf16 v[104:107], v[160:163], v[64:67], 0\n"
    "ds_read_b128 v[188:191], v223 offset:12288\n"
    "s_bitcmp1_b32 s79, 15\n"
    "v_mfma_f32_16x16x32_bf16 v[104:107], v[164:167], v[68:71], v[104:107]\n"
    "s_cselect_b32 s75, s85, s75\n"
    "s_add_u32 s73, s92, 1\n"
    "s_sub_u32 s78, s77, 1\n"
    "s_waitcnt lgkmcnt(4)\n"
    "v_mfma_f32_16x16x32_bf16 v[104:107], v[168:171], v[72:75], v[104:107]\n"
    "ds_read_b128 v[192:195], v222 offset:6144\n"
    "s_cmp_lg_u32 s88, 0\n"
    "v_mfma_f32_16x16x32_bf16 v[104:107], v[172:175], v[76:79], v[104:107]\n"
    "s_cselect_b32 s84, 2, 0\n"
    "s_cselect_b32 s72, PQ_%=-BASE_%=, s72\n"
    "s_cselect_b32 s72, s72, P_%=-BASE_%=\n"
    "v_mfma_f32_16x16x32_bf16 v[108:111], v[160:163], v[80:83], 0\n"
    "ds_read_b128 v[196:199], v223 offset:6144\n"
    "s_cmp_lg_u32 s85, 0\n"
    "s_cselect_b32 s72, G_%=-BASE_%=, s72\n"
    "v_mfma_f32_16x16x32_bf16 v[108:111], v[164:167], v[84:87], v[108:111]\n"
    "s_cselect_b32 s74, 0, s79\n"
    "s_cmp_eq_u32 s73, s77\n"
    "s_cselect_b32 s78, s77, s78\n"
    "v_mfma_f32_16x16x32_bf16 v[108:111], v[168:171], v[88:91], v[108:111]\n"
    "ds_read_b128 v[200:203], v222 offset:14336\n"
    "s_cselect_b32 s84, s84, 1\n"
    "s_cselect_b32 s72, s72, s96\n"
    "v_mfma_f32_16x16x32_bf16 v[108:111], v[172:175], v[92:95], v[108:111]\n"
    "s_add_u32 s54, s56, s72\n"
    "s_addc_u32 s55, s57, 0\n"
    "s_bitcmp1_b32 s74, 16\n"
    "s_waitcnt lgkmcnt(5)\n"
    "v_mfma_f32_16x16x32_bf16 v[112:115], v[176:179], v[64:67], 0\n"
    "ds_read_b128 v[204:207], v223 offset:14336\n"
    "s_cselect_b64 s[50:51], s[52:53], s[54:55]\n"
    "v_mfma_f32_16x16x32_bf16 v[112:115], v[180:183], v[68:71], v[112:115]\n"
    "s_cmp_eq_u32 s72, G_%=-BASE_%=\n"
    "s_cselect_b32 s75, 1, s75\n"
    "s_cmp_eq_u32 s84, 0\n"
    "s_waitcnt lgkmcnt(4)\n"
    "v_mfma_f32_16x16x32_bf16 v[112:115], v[184:187], v[72:75], v[112:115]\n"
    "s_cselect_b32 s75, 1, s75\n"
    "s_cmp_lg_u32 s75, 0\n"
    "v_mfma_f32_16x16x32_bf16 v[112:115], v[188:191], v[76:79], v[112:115]\n"
    "s_cselect_b32 s93, s73, s93\n"
    "s_mov_b32 s96, F_%=-BASE_%=\n"
    "s_mov_b32 s94, 1\n"
    "v_mfma_f32_16x16x32_bf16 v[116:119], v[176:179], v[80:83], 0\n"
    "s_cmp_eq_u32 s72, PQ_%=-BASE_%=\n"
    "v_mfma_f32_16x16x32_bf16 v[116:119], v[180:183], v[84:87], v[116:119]\n"
    "v_mfma_f32_16x16x32_bf16 v[116:119], v[184:187], v[88:91], v[116:119]\n"
    "v_mfma_f32_16x16x32_bf16 v[116:119], v[188:191], v[92:95], v[116:119]\n"
    "s_waitcnt lgkmcnt(2)\n"
    "v_mfma_f32_16x16x32_bf16 v[120:123], v[192:195], v[64:67], 0\n"
    "v_mfma_f32_16x16x32_bf16 v[120:123], v[196:199], v[68:71], v[120:123]\n"
    "s_waitcnt lgkmcnt(0)\n"
    "v_mfma_f32_16x16x32_bf16 v[120:123], v[200:203], v[72:75], v[120:123]\n"
    "v_mfma_f32_16x16x32_bf16 v[120:123], v[204:207], v[76:79], v[120:123]\n"
    "v_mfma_f32_16x16x32_bf16 v[124:127], v[192:195], v[80:83], 0\n"
    "v_readlane_b32 s98, v233, s92\n"
    "v_mfma_f32_16x16x32_bf16 v[124:127], v[196:199], v[84:87], v[124:127]\n"
    "s_cbranch_vccz QK0B_%=\n"
    "s_waitcnt vmcnt(5)\n"
    "QK0B_%=:\n"
    "s_barrier\n"
    "v_mfma_f32_16x16x32_bf16 v[124:127], v[200:203], v[88:91], v[124:127]\n"
    "v_mfma_f32_16x16x32_bf16 v[124:127], v[204:207], v[92:95], v[124:127]\n"
    "s_setprio 0\n"
    "s_branch G_%=\n"
    ".p2align 6\n"
    "LOOP_%=:\n"
    "s_setprio 1\n"
    "v_mfma_f32_16x16x32_bf16 v[208:211], v[216:219], v[128:131], v[208:211]\n"
    "s_cmp_eq_u32 s92, s66\n"
    "ds_read_b64_tr_b16 v[164:165], v224 offset:8704\n"
    "s_cbranch_scc1 SEGL_%=\n"
    "v_mfma_f32_16x16x32_bf16 v[208:211], v[216:219], v[136:139], v[208:211]\n"
    "ds_read_b64_tr_b16 v[166:167], v224 offset:12800\n"
    "v_mfma_f32_16x16x32_bf16 v[212:215], v[216:219], v[132:135], v[212:215]\n"
    "ds_read_b64_tr_b16 v[168:169], v225 offset:512\n"
    "v_mfma_f32_16x16x32_bf16 v[212:215], v[216:219], v[140:143], v[212:215]\n"
    "ds_read_b64_tr_b16 v[170:171], v225 offset:4608\n"
    "v_mfma_f32_16x16x32_bf16 v[0:3], v[144:147], v[128:131], v[0:3]\n"
    "ds_read_b64_tr_b16 v[172:173], v225 offset:8704\n"
    "v_mfma_f32_16x16x32_bf16 v[0:3], v[148:151], v[136:139], v[0:3]\n"
    "ds_read_b64_tr_b16 v[174:175], v225 offset:12800\n"
    "v_mfma_f32_16x16x32_bf16 v[4:7], v[144:147], v[132:135], v[4:7]\n"
    "ds_read_b64_tr_b16 v[176:177], v224 offset:1024\n"
    "RSEGL_%=:\n"
    "v_mfma_f32_16x16x32_bf16 v[4:7], v[148:151], v[140:143], v[4:7]\n"
    "ds_read_b64_tr_b16 v[178:179], v224 offset:5120\n"
    "v_mfma_f32_16x16x32_bf16 v[8:11], v[152:155], v[128:131], v[8:11]\n"
    "ds_read_b64_tr_b16 v[180:181], v224 offset:9216\n"
    "v_mfma_f32_16x16x32_bf16 v[8:11], v[156:159], v[136:139], v[8:11]\n"
    "ds_read_b64_tr_b16 v[182:183], v224 offset:13312\n"
    "v_mfma_f32_16x16x32_bf16 v[12:15], v[152:155], v[132:135], v[12:15]\n"
    "ds_read_b64_tr_b16 v[184:185], v225 offset:1024\n"
    "v_mfma_f32_16x16x32_bf16 v[12:15], v[156:159], v[140:143], v[12:15]\n"
    "ds_read_b64_tr_b16 v[186:187], v225 offset:5120\n"
    "v_mfma_f32_16x16x32_bf16 v[16:19], v[160:163], v[128:131], v[16:19]\n"
    "ds_read_b64_tr_b16 v[188:189], v225 offset:9216\n"
    "s_waitcnt lgkmcnt(11)\n"
    "v_mfma_f32_16x16x32_bf16 v[16:19], v[164:167], v[136:139], v[16:19]\n"
    "ds_read_b64_tr_b16 v[190:191], v225 offset:13312\n"
    "v_mfma_f32_16x16x32_bf16 v[20:23], v[160:163], v[132:135], v[20:23]\n"
    "ds_read_b64_tr_b16 v[192:193], v224 offset:1536\n"
    "v_mfma_f32_16x16x32_bf16 v[20:23], v[164:167], v[140:143], v[20:23]\n"
    "ds_read_b64_tr_b16 v[194:195], v224 offset:5632\n"
    "s_waitcnt lgkmcnt(10)\n"
    "v_mfma_f32_16x16x32_bf16 v[24:27], v[168:171], v[128:131], v[24:27]\n"
    "ds_read_b64_tr_b16 v[196:197], v224 offset:9728\n"
    "v_mfma_f32_16x16x32_bf16 v[24:27], v[172:175], v[136:139], v[24:27]\n"
    "ds_read_b64_tr_b16 v[198:199], v224 offset:13824\n"
    "v_mfma_f32_16x16x32_bf16 v[28:31], v[168:171], v[132:135], v[28:31]\n"
    "ds_read_b64_tr_b16 v[200:201], v225 offset:1536\n"
    "v_mfma_f32_16x16x32_bf16 v[28:31], v[172:175], v[140:143], v[28:31]\n"
    "ds_read_b64_tr_b16 v[202:203], v225 offset:5632\n"
    "s_waitcnt lgkmcnt(10)\n"
    "v_mfma_f32_16x16x32_bf16 v[32:35], v[176:179], v[128:131], v[32:35]\n"
    "ds_read_b64_tr_b16 v[204:205], v225 offset:9728\n"
    "v_mfma_f32_16x16x32_bf16 v[32:35], v[180:183], v[136:139], v[32:35]\n"
    "ds_read_b64_tr_b16 v[206:207], v225 offset:13824\n"
    "v_mfma_f32_16x16x32_bf16 v[36:39], v[176:179], v[132:135], v[36:39]\n"
    "ds_read_b128 v[144:147], v222 offset:0\n"
    "v_mfma_f32_16x16x32_bf16 v[36:39], v[180:183], v[140:143], v[36:39]\n"
    "s_waitcnt lgkmcnt(9)\n"
    "v_mfma_f32_16x16x32_bf16 v[40:43], v[184:187], v[128:131], v[40:43]\n"
    "ds_read_b128 v[148:151], v223 offset:0\n"
    "v_mfma_f32_16x16x32_bf16 v[40:43], v[188:191], v[136:139], v[40:43]\n"
    "v_mfma_f32_16x16x32_bf16 v[44:47], v[184:187], v[132:135], v[44:47]\n"
    "ds_read_b128 v[152:155], v222 offset:8192\n"
    "v_mfma_f32_16x16x32_bf16 v[44:47], v[188:191], v[140:143], v[44:47]\n"
    "s_waitcnt lgkmcnt(7)\n"
    "v_mfma_f32_16x16x32_bf16 v[48:51], v[192:195], v[128:131], v[48:51]\n"
    "ds_read_b128 v[156:159], v223 offset:8192\n"
    "v_mfma_f32_16x16x32_bf16 v[48:51], v[196:199], v[136:139], v[48:51]\n"
    "v_mfma_f32_16x16x32_bf16 v[52:55], v[192:195], v[132:135], v[52:55]\n"
    "s_cmp_lg_u32 s84, 1\n"
    "ds_read_b128 v[160:163], v222 offset:2048\n"
    "v_mfma_f32_16x16x32_bf16 v[52:55], v[196:199], v[140:143], v[52:55]\n"
    "s_waitcnt lgkmcnt(5)\n"
    "v_mfma_f32_16x16x32_bf16 v[56:59], v[200:203], v[128:131], v[56:59]\n"
    "ds_read_b128 v[164:167], v223 offset:2048\n"
    "v_mfma_f32_16x16x32_bf16 v[56:59], v[204:207], v[136:139], v[56:59]\n"
    "v_mfma_f32_16x16x32_bf16 v[60:63], v[200:203], v[132:135], v[60:63]\n"
    "ds_read_b128 v[168:171], v222 offset:10240\n"
    "v_mfma_f32_16x16x32_bf16 v[60:63], v[204:207], v[140:143], v[60:63]\n"
    "QKSEL_%=:\n"
    "s_cbranch_scc1 QKX_%=\n"
    "QK_%=:\n"
    "s_waitcnt lgkmcnt(5)\n"
    "v_mfma_f32_16x16x32_bf16 v[96:99], v[144:147], v[64:67], 0\n"
    "s_cmp_eq_u32 s92, s78\n"
    "ds_read_b128 v[172:175], v223 offset:10240\n"
    "s_cbranch_scc1 SEGC_%=\n"
    "v_mfma_f32_16x16x32_bf16 v[96:99], v[148:151], v[68:71], v[96:99]\n"
    "v_add_u32 v224, s86, %[vb0]\n"
    "s_waitcnt lgkmcnt(4)\n"
    "v_mfma_f32_16x16x32_bf16 v[96:99], v[152:155], v[72:75], v[96:99]\n"
    "ds_read_b128 v[176:179], v222 offset:4096\n"
    "v_mfma_f32_16x16x32_bf16 v[96:99], v[156:159], v[76:79], v[96:99]\n"
    "v_xor_b32 v225, 32, v224\n"
    "v_mfma_f32_16x16x32_bf16 v[100:103], v[144:147], v[80:83], 0\n"
    "ds_read_b128 v[180:183], v223 offset:4096\n"
    "v_mfma_f32_16x16x32_bf16 v[100:103], v[148:151], v[84:87], v[100:103]\n"
    "v_mfma_f32_16x16x32_bf16 v[100:103], v[152:155], v[88:91], v[100:103]\n"
    "ds_read_b128 v[184:187], v222 offset:12288\n"
    "v_mfma_f32_16x16x32_bf16 v[100:103], v[156:159], v[92:95], v[100:103]\n"
    "s_waitcnt lgkmcnt(5)\n"
    "v_mfma_f32_16x16x32_bf16 v[104:107], v[160:163], v[64:67], 0\n"
    "ds_read_b128 v[188:191], v223 offset:12288\n"
    "v_mfma_f32_16x16x32_bf16 v[104:107], v[164:167], v[68:71], v[104:107]\n"
    "s_waitcnt lgkmcnt(4)\n"
    "v_mfma_f32_16x16x32_bf16 v[104:107], v[168:171], v[72:75], v[104:107]\n"
    "ds_read_b128 v[192:195], v222 offset:6144\n"
    "v_mfma_f32_16x16x32_bf16 v[104:107], v[172:175], v[76:79], v[104:107]\n"
    "v_mfma_f32_16x16x32_bf16 v[108:111], v[160:163], v[80:83], 0\n"
    "ds_read_b128 v[196:199], v223 offset:6144\n"
    "v_mfma_f32_16x16x32_bf16 v[108:111], v[164:167], v[84:87], v[108:111]\n"
    "v_mfma_f32_16x16x32_bf16 v[108:111], v[168:171], v[88:91], v[108:111]\n"
    "ds_read_b128 v[200:203], v222 offset:14336\n"
    "v_mfma_f32_16x16x32_bf16 v[108:111], v[172:175], v[92:95], v[108:111]\n"
    "s_waitcnt lgkmcnt(5)\n"
    "v_mfma_f32_16x16x32_bf16 v[112:115], v[176:179], v[64:67], 0\n"
    "ds_read_b128 v[204:207], v223 offset:14336\n"
    "v_mfma_f32_16x16x32_bf16 v[112:115], v[180:183], v[68:71], v[112:115]\n"
    "s_waitcnt lgkmcnt(4)\n"
    "v_mfma_f32_16x16x32_bf16 v[112:115], v[184:187], v[72:75], v[112:115]\n"
    "v_mfma_f32_16x16x32_bf16 v[112:115], v[188:191], v[76:79], v[112:115]\n"
    "v_mfma_f32_16x16x32_bf16 v[116:119], v[176:179], v[80:83], 0\n"
    "RSEGC_%=:\n"
    "s_cbranch_scc1 SEGQ_%=\n"
    "v_mfma_f32_16x16x32_bf16 v[116:119], v[180:183], v[84:87], v[116:119]\n"
    "v_mfma_f32_16x16x32_bf16 v[116:119], v[184:187], v[88:91], v[116:119]\n"
    "ds_read_b64_tr_b16 v[144:145], v224 offset:0\n"
    "v_mfma_f32_16x16x32_bf16 v[116:119], v[188:191], v[92:95], v[116:119]\n"
    "ds_read_b64_tr_b16 v[146:147], v224 offset:4096\n"
    "s_waitcnt lgkmcnt(4)\n"
    "v_mfma_f32_16x16x32_bf16 v[120:123], v[192:195], v[64:67], 0\n"
    "ds_read_b64_tr_b16 v[148:149], v224 offset:8192\n"
    "v_mfma_f32_16x16x32_bf16 v[120:123], v[196:199], v[68:71], v[120:123]\n"
    "ds_read_b64_tr_b16 v[150:151], v224 offset:12288\n"
    "s_waitcnt lgkmcnt(4)\n"
    "v_mfma_f32_16x16x32_bf16 v[120:123], v[200:203], v[72:75], v[120:123]\n"
    "ds_read_b64_tr_b16 v[152:153], v225 offset:0\n"
    "v_mfma_f32_16x16x32_bf16 v[120:123], v[204:207], v[76:79], v[120:123]\n"
    "ds_read_b64_tr_b16 v[154:155], v225 offset:4096\n"
    "RSEGQ_%=:\n"
    "v_mfma_f32_16x16x32_bf16 v[124:127], v[192:195], v[80:83], 0\n"
    "v_readlane_b32 s98, v233, s92\n"
    "ds_read_b64_tr_b16 v[156:157], v225 offset:8192\n"
    "v_mfma_f32_16x16x32_bf16 v[124:127], v[196:199], v[84:87], v[124:127]\n"
    "s_cmp_lg_u32 s94, 0\n"
    "ds_read_b64_tr_b16 v[158:159], v225 offset:12288\n"
    "s_barrier\n"
    "v_mfma_f32_16x16x32_bf16 v[124:127], v[200:203], v[88:91], v[124:127]\n"
    "ds_read_b64_tr_b16 v[160:161], v224 offset:512\n"
    "v_mfma_f32_16x16x32_bf16 v[124:127], v[204:207], v[92:95], v[124:127]\n"
    "ds_read_b64_tr_b16 v[162:163], v224 offset:4608\n"
    "s_setprio 0\n"
    "s_cbranch_scc1 DISP_%=\n"
    "P_%=:\n"
    "v_fma_f32 v176, v96, s64, v220\n"
    "v_fma_f32 v177, v97, s64, v220\n"
    "v_fma_f32 v178, v98, s64, v220\n"
    "s_add_u32 m0, s68, s70\n"
    "v_fma_f32 v179, v99, s64, v220\n"
    "v_exp_f32 v176, v176\n"
    "buffer_load_dwordx4 %[ksrc], s[28:31], s67 offen lds\n"
    "v_fma_f32 v180, v100, s64, v221\n"
    "v_exp_f32 v177, v177\n"
    "v_fma_f32 v181, v101, s64, v221\n"
    "v_exp_f32 v178, v178\n"
    "v_fma_f32 v182, v102, s64, v221\n"
    "v_exp_f32 v179, v179\n"
    "v_fma_f32 v183, v103, s64, v221\n"
    "v_exp_f32 v180, v180\n"
    "v_fma_f32 v184, v104, s64, v220\n"
    "v_exp_f32 v181, v181\n"
    "v_fma_f32 v185, v105, s64, v220\n"
    "v_cvt_pk_bf16_f32 v128, v176, v177\n"
    "s_add_u32 m0, m0, s90\n"
    "v_exp_f32 v182, v182\n"
    "v_fma_f32 v186, v106, s64, v220\n"
    "buffer_load_dwordx4 %[ksrc], s[28:31], s67 offen offset:128 lds\n"
    "v_exp_f32 v183, v183\n"
    "v_fma_f32 v187, v107, s64, v220\n"
    "v_cvt_pk_bf16_f32 v129, v178, v179\n"
    "v_exp_f32 v184, v184\n"
    "v_fma_f32 v188, v108, s64, v221\n"
    "v_or_b32 v230, v128, v129\n"
    "v_exp_f32 v185, v185\n"
    "v_fma_f32 v189, v109, s64, v221\n"
    "v_cvt_pk_bf16_f32 v132, v180, v181\n"
    "v_exp_f32 v186, v186\n"
    "v_fma_f32 v190, v110, s64, v221\n"
    "v_exp_f32 v187, v187\n"
    "s_add_u32 m0, s68, s71\n"
    "v_fma_f32 v191, v111, s64, v221\n"
    "s_add_u32 s68, s68, 32768\n"
    "v_cvt_pk_bf16_f32 v133, v182, v183\n"
    "buffer_load_dwordx4 %[vsrc], s[32:35], s67 offen lds\n"
    "v_exp_f32 v188, v188\n"
    "v_fma_f32 v176, v112, s64, v220\n"
    "v_or3_b32 v230, v230, v132, v133\n"
    "v_exp_f32 v189, v189\n"
    "v_fma_f32 v177, v113, s64, v220\n"
    "v_cvt_pk_bf16_f32 v130, v184, v185\n"
    "v_exp_f32 v190, v190\n"
    "v_fma_f32 v178, v114, s64, v220\n"
    "v_exp_f32 v191, v191\n"
    "v_fma_f32 v179, v115, s64, v220\n"
    "v_cvt_pk_bf16_f32 v131, v186, v187\n"
    "v_exp_f32 v176, v176\n"
    "s_add_u32 m0, m0, 896\n"
    "v_fma_f32 v180, v116, s64, v221\n"
    "s_and_b32 s68, s68, s69\n"
    "v_or3_b32 v230, v230, v130, v131\n"
    "buffer_load_dwordx4 %[vsrc], s[32:35], s67 offen offset:128 lds\n"
    "v_exp_f32 v177, v177\n"
    "s_add_u32 s67, s67, 65536\n"
    "v_fma_f32 v181, v117, s64, v221\n"
    "v_cvt_pk_bf16_f32 v134, v188, v189\n"
    "v_exp_f32 v178, v178\n"
    "v_fma_f32 v182, v118, s64, v221\n"
    "v_exp_f32 v179, v179\n"
    "v_fma_f32 v183, v119, s64, v221\n"
    "v_cvt_pk_bf16_f32 v135, v190, v191\n"
    "v_exp_f32 v180, v180\n"
    "v_fma_f32 v184, v120, s64, v220\n"
    "v_or3_b32 v230, v230, v134, v135\n"
    "v_exp_f32 v181, v181\n"
    "buffer_load_dword v232, %[qpf], s[36:39], s98 offen\n"
    "v_fma_f32 v185, v121, s64, v220\n"
    "v_cvt_pk_bf16_f32 v136, v176, v177\n"
    "s_add_u32 s86, s86, 32768\n"
    "v_exp_f32 v182, v182\n"
    "s_and_b32 s86, s86, 0x1ffff\n"
    "v_fma_f32 v186, v122, s64, v220\n"
    "s_add_u32 s92, s92, 1\n"
    "v_exp_f32 v183, v183\n"
    "v_add_u32 v222, s86, %[kb0]\n"
    "v_fma_f32 v187, v123, s64, v220\n"
    "v_xor_b32 v223, 64, v222\n"
    "v_cvt_pk_bf16_f32 v137, v178, v179\n"
    "s_mov_b32 s94, 0\n"
    "v_exp_f32 v184, v184\n"
    "v_fma_f32 v188, v124, s64, v221\n"
    "v_or3_b32 v230, v230, v136, v137\n"
    "v_exp_f32 v185, v185\n"
    "v_fma_f32 v189, v125, s64, v221\n"
    "v_cvt_pk_bf16_f32 v140, v180, v181\n"
    "v_exp_f32 v186, v186\n"
    "v_fma_f32 v190, v126, s64, v221\n"
    "v_exp_f32 v187, v187\n"
    "v_fma_f32 v191, v127, s64, v221\n"
    "v_cvt_pk_bf16_f32 v141, v182, v183\n"
    "v_exp_f32 v188, v188\n"
    "v_or3_b32 v230, v230, v140, v141\n"
    "v_exp_f32 v189, v189\n"
    "v_cvt_pk_bf16_f32 v138, v184, v185\n"
    "v_exp_f32 v190, v190\n"
    "v_exp_f32 v191, v191\n"
    "v_cvt_pk_bf16_f32 v139, v186, v187\n"
    "v_or3_b32 v230, v230, v138, v139\n"
    "v_cvt_pk_bf16_f32 v142, v188, v189\n"
    "v_cvt_pk_bf16_f32 v143, v190, v191\n"
    "v_or3_b32 v230, v230, v142, v143\n"
    "s_cmp_eq_u32 s92, s93\n"
    "v_and_b32 v230, 0x40004000, v230\n"
    "v_cmp_ne_u32 vcc, 0, v230\n"
    "s_cbranch_vccnz RARE_%=\n"
    "TAIL_%=:\n"
    "s_waitcnt vmcnt(6) lgkmcnt(0)\n"
    "s_cbranch_scc1 POST_%=\n"
    "s_barrier\n"
    "s_branch LOOP_%=\n"
    "DISP_%=:\n"
    "s_setpc_b64 s[50:51]\n"
    "PQ_%=:\n"
    "v_fma_f32 v176, v96, s64, v220\n"
    "v_fma_f32 v177, v97, s64, v220\n"
    "v_fma_f32 v178, v98, s64, v220\n"
    "v_fma_f32 v179, v99, s64, v220\n"
    "v_exp_f32 v176, v176\n"
    "s_add_u32 s86, s86, 32768\n"
    "v_fma_f32 v180, v100, s64, v221\n"
    "s_and_b32 s86, s86, 0x1ffff\n"
    "v_exp_f32 v177, v177\n"
    "s_add_u32 s92, s92, 1\n"
    "v_fma_f32 v181, v101, s64, v221\n"
    "v_add_u32 v222, s86, %[kb0]\n"
    "v_exp_f32 v178, v178\n"
    "v_xor_b32 v223, 64, v222\n"
    "v_fma_f32 v182, v102, s64, v221\n"
    "s_mov_b32 s94, 0\n"
    "v_exp_f32 v179, v179\n"
    "v_fma_f32 v183, v103, s64, v221\n"
    "v_exp_f32 v180, v180\n"
    "v_fma_f32 v184, v104, s64, v220\n"
    "v_exp_f32 v181, v181\n"
    "v_fma_f32 v185, v105, s64, v220\n"
    "v_cvt_pk_bf16_f32 v128, v176, v177\n"
    "v_exp_f32 v182, v182\n"
    "v_fma_f32 v186, v106, s64, v220\n"
    "v_exp_f32 v183, v183\n"
    "v_fma_f32 v187, v107, s64, v220\n"
    "v_cvt_pk_bf16_f32 v129, v178, v179\n"
    "v_exp_f32 v184, v184\n"
    "s_waitcnt vmcnt(0)\n"
    "v_fma_f32 v188, v108, s64, v221\n"
    "ds_read_b128 v[64:67], %[sa] offset:0\n"
    "v_or_b32 v230, v128, v129\n"
    "ds_read_b128 v[68:71], %[sa] offset:64\n"
    "v_exp_f32 v185, v185\n"
    "ds_read_b128 v[72:75], %[sa] offset:128\n"
    "v_fma_f32 v189, v109, s64, v221\n"
    "ds_read_b128 v[76:79], %[sa] offset:192\n"
    "v_cvt_pk_bf16_f32 v132, v180, v181\n"
    "v_exp_f32 v186, v186\n"
    "v_fma_f32 v190, v110, s64, v221\n"
    "v_exp_f32 v187, v187\n"
    "v_fma_f32 v191, v111, s64, v221\n"
    "v_cvt_pk_bf16_f32 v133, v182, v183\n"
    "v_exp_f32 v188, v188\n"
    "v_fma_f32 v176, v112, s64, v220\n"
    "v_or3_b32 v230, v230, v132, v133\n"
    "s_waitcnt lgkmcnt(0)\n"
    "v_exp_f32 v189, v189\n"
    "s_add_u32 m0, s82, 0\n"
    "v_fma_f32 v177, v113, s64, v220\n"
    "s_add_u32 s72, s89, 16384\n"
    "v_cvt_pk_bf16_f32 v130, v184, v185\n"
    "buffer_load_dwordx4 %[qs], s[36:39], s72 offen lds\n"
    "v_exp_f32 v190, v190\n"
    "v_fma_f32 v178, v114, s64, v220\n"
    "v_exp_f32 v191, v191\n"
    "s_add_u32 m0, s82, 1024\n"
    "v_fma_f32 v179, v115, s64, v220\n"
    "s_add_u32 s72, s89, 20480\n"
    "v_cvt_pk_bf16_f32 v131, v186, v187\n"
    "buffer_load_dwordx4 %[qs], s[36:39], s72 offen lds\n"
    "v_exp_f32 v176, v176\n"
    "v_fma_f32 v180, v116, s64, v221\n"
    "v_or3_b32 v230, v230, v130, v131\n"
    "s_add_u32 m0, s82, 2048\n"
    "v_exp_f32 v177, v177\n"
    "s_add_u32 s72, s89, 24576\n"
    "v_fma_f32 v181, v117, s64, v221\n"
    "buffer_load_dwordx4 %[qs], s[36:39], s72 offen lds\n"
    "v_cvt_pk_bf16_f32 v134, v188, v189\n"
    "v_exp_f32 v178, v178\n"
    "v_fma_f32 v182, v118, s64, v221\n"
    "s_add_u32 m0, s82, 3072\n"
    "v_exp_f32 v179, v179\n"
    "s_add_u32 s72, s89, 28672\n"
    "v_fma_f32 v183, v119, s64, v221\n"
    "buffer_load_dwordx4 %[qs], s[36:39], s72 offen lds\n"
    "v_cvt_pk_bf16_f32 v135, v190, v191\n"
    "v_exp_f32 v180, v180\n"
    "v_fma_f32 v184, v120, s64, v220\n"
    "s_add_u32 m0, s68, s70\n"
    "v_or3_b32 v230, v230, v134, v135\n"
    "v_exp_f32 v181, v181\n"
    "buffer_load_dwordx4 %[ksrc], s[28:31], s67 offen lds\n"
    "v_fma_f32 v185, v121, s64, v220\n"
    "v_cvt_pk_bf16_f32 v136, v176, v177\n"
    "v_exp_f32 v182, v182\n"
    "s_add_u32 m0, m0, s90\n"
    "v_fma_f32 v186, v122, s64, v220\n"
    "v_exp_f32 v183, v183\n"
    "buffer_load_dwordx4 %[ksrc], s[28:31], s67 offen offset:128 lds\n"
    "v_fma_f32 v187, v123, s64, v220\n"
    "v_cvt_pk_bf16_f32 v137, v178, v179\n"
    "v_exp_f32 v184, v184\n"
    "s_add_u32 m0, s68, s71\n"
    "v_fma_f32 v188, v124, s64, v221\n"
    "s_add_u32 s68, s68, 32768\n"
    "v_or3_b32 v230, v230, v136, v137\n"
    "buffer_load_dwordx4 %[vsrc], s[32:35], s67 offen lds\n"
    "v_exp_f32 v185, v185\n"
    "v_fma_f32 v189, v125, s64, v221\n"
    "v_cvt_pk_bf16_f32 v140, v180, v181\n"
    "s_add_u32 m0, m0, 896\n"
    "v_exp_f32 v186, v186\n"
    "s_and_b32 s68, s68, s69\n"
    "v_fma_f32 v190, v126, s64, v221\n"
    "buffer_load_dwordx4 %[vsrc], s[32:35], s67 offen offset:128 lds\n"
    "v_exp_f32 v187, v187\n"
    "s_add_u32 s67, s67, 65536\n"
    "v_fma_f32 v191, v127, s64, v221\n"
    "v_cvt_pk_bf16_f32 v141, v182, v183\n"
    "buffer_load_dword v232, %[qpf], s[36:39], s98 offen\n"
    "v_exp_f32 v188, v188\n"
    "v_or3_b32 v230, v230, v140, v141\n"
    "v_exp_f32 v189, v189\n"
    "v_cvt_pk_bf16_f32 v138, v184, v185\n"
    "v_exp_f32 v190, v190\n"
    "v_exp_f32 v191, v191\n"
    "v_cvt_pk_bf16_f32 v139, v186, v187\n"
    "v_or3_b32 v230, v230, v138, v139\n"
    "v_cvt_pk_bf16_f32 v142, v188, v189\n"
    "v_cvt_pk_bf16_f32 v143, v190, v191\n"
    "v_or3_b32 v230, v230, v142, v143\n"
    "s_cmp_eq_u32 s92, s93\n"
    "v_and_b32 v230, 0x40004000, v230\n"
    "v_cmp_ne_u32 vcc, 0, v230\n"
    "s_cbranch_vccnz RARE_%=\n"
    "s_waitcnt vmcnt(9) lgkmcnt(0)\n"
    "s_cbranch_scc1 POST_%=\n"
    "s_barrier\n"
    "s_branch LOOP_%=\n"
    ".p2align 6\n"
    "F_%=:\n"
    "v_rcp_f32 v228, v208\n"
    "v_rcp_f32 v230, v212\n"
    "v_mov_b32 v209, v212\n"
    "v_mov_b32 v210, v220\n"
    "v_mov_b32 v211, v221\n"
    "v_max3_f32 v192, v96, v97, v98\n"
    "s_add_u32 m0, s68, s70\n"
    "v_max3_f32 v193, v100, v101, v102\n"
    "v_max3_f32 v192, v192, v99, v104\n"
    "buffer_load_dwordx4 %[ksrc], s[28:31], s67 offen lds\n"
    "v_max3_f32 v193, v193, v103, v108\n"
    "v_max3_f32 v192, v192, v105, v106\n"
    "v_max3_f32 v193, v193, v109, v110\n"
    "v_max3_f32 v192, v192, v107, v112\n"
    "v_max3_f32 v193, v193, v111, v116\n"
    "v_max3_f32 v192, v192, v113, v114\n"
    "v_max3_f32 v193, v193, v117, v118\n"
    "v_max3_f32 v192, v192, v115, v120\n"
    "v_max3_f32 v193, v193, v119, v124\n"
    "v_max3_f32 v192, v192, v121, v122\n"
    "v_max3_f32 v193, v193, v125, v126\n"
    "v_max_f32 v192, v192, v123\n"
    "buffer_store_dwordx4 v[208:211], %[woff], s[44:47], s83 offen sc1\n"
    "v_max_f32 v193, v193, v127\n"
    "v_mov_b32 v194, v192\n"
    "v_mov_b32 v195, v193\n"
    "s_nop 0\n"
    "v_permlane16_swap_b32 v192, v194\n"
    "v_permlane16_swap_b32 v193, v195\n"
    "v_max_f32 v192, v192, v194\n"
    "v_max_f32 v193, v193, v195\n"
    "v_mov_b32 v194, v192\n"
    "v_mov_b32 v195, v193\n"
    "s_nop 0\n"
    "v_permlane32_swap_b32 v192, v194\n"
    "v_permlane32_swap_b32 v193, v195\n"
    "v_max_f32 v192, v192, v194\n"
    "s_add_u32 m0, m0, s90\n"
    "v_max_f32 v193, v193, v195\n"
    "v_mul_f32 v192, s64, v192\n"
    "buffer_load_dwordx4 %[ksrc], s[28:31], s67 offen offset:128 lds\n"
    "v_mul_f32 v193, s64, v193\n"
    "v_sub_f32 v192, 0xc1400000, v192\n"
    "v_sub_f32 v193, 0xc1400000, v193\n"
    "v_mov_b32 v220, v192\n"
    "v_mov_b32 v221, v193\n"
    "v_fma_f32 v176, v96, s64, v220\n"
    "v_fma_f32 v177, v97, s64, v220\n"
    "v_fma_f32 v178, v98, s64, v220\n"
    "v_fma_f32 v179, v99, s64, v220\n"
    "v_exp_f32 v176, v176\n"
    "v_fma_f32 v180, v100, s64, v221\n"
    "v_exp_f32 v177, v177\n"
    "s_add_u32 m0, s68, s71\n"
    "v_fma_f32 v181, v101, s64, v221\n"
    "s_add_u32 s68, s68, 32768\n"
    "v_exp_f32 v178, v178\n"
    "buffer_load_dwordx4 %[vsrc], s[32:35], s67 offen lds\n"
    "v_fma_f32 v182, v102, s64, v221\n"
    "v_exp_f32 v179, v179\n"
    "v_fma_f32 v183, v103, s64, v221\n"
    "v_exp_f32 v180, v180\n"
    "v_fma_f32 v184, v104, s64, v220\n"
    "v_exp_f32 v181, v181\n"
    "v_fma_f32 v185, v105, s64, v220\n"
    "v_cvt_pk_bf16_f32 v128, v176, v177\n"
    "v_exp_f32 v182, v182\n"
    "v_fma_f32 v186, v106, s64, v220\n"
    "v_exp_f32 v183, v183\n"
    "v_fma_f32 v187, v107, s64, v220\n"
    "s_add_u32 m0, m0, 896\n"
    "v_cvt_pk_bf16_f32 v129, v178, v179\n"
    "s_and_b32 s68, s68, s69\n"
    "v_exp_f32 v184, v184\n"
    "buffer_load_dwordx4 %[vsrc], s[32:35], s67 offen offset:128 lds\n"
    "v_fma_f32 v188, v108, s64, v221\n"
    "s_add_u32 s67, s67, 65536\n"
    "v_exp_f32 v185, v185\n"
    "v_fma_f32 v189, v109, s64, v221\n"
    "v_cvt_pk_bf16_f32 v132, v180, v181\n"
    "v_exp_f32 v186, v186\n"
    "v_fma_f32 v190, v110, s64, v221\n"
    "v_exp_f32 v187, v187\n"
    "v_fma_f32 v191, v111, s64, v221\n"
    "v_cvt_pk_bf16_f32 v133, v182, v183\n"
    "v_exp_f32 v188, v188\n"
    "v_fma_f32 v176, v112, s64, v220\n"
    "v_exp_f32 v189, v189\n"
    "v_fma_f32 v177, v113, s64, v220\n"
    "v_cvt_pk_bf16_f32 v130, v184, v185\n"
    "v_exp_f32 v190, v190\n"
    "v_fma_f32 v178, v114, s64, v220\n"
    "v_exp_f32 v191, v191\n"
    "s_add_u32 s86, s86, 32768\n"
    "v_fma_f32 v179, v115, s64, v220\n"
    "s_and_b32 s86, s86, 0x1ffff\n"
    "v_cvt_pk_bf16_f32 v131, v186, v187\n"
    "s_add_u32 s92, s92, 1\n"
    "v_exp_f32 v176, v176\n"
    "v_add_u32 v222, s86, %[kb0]\n"
    "v_fma_f32 v180, v116, s64, v221\n"
    "v_xor_b32 v223, 64, v222\n"
    "v_mul_f32 v0, v0, v228\n"
    "s_mov_b32 s94, 0\n"
    "v_mul_f32 v1, v1, v228\n"
    "v_mul_f32 v2, v2, v228\n"
    "v_mul_f32 v3, v3, v228\n"
    "v_mul_f32 v8, v8, v228\n"
    "v_mul_f32 v9, v9, v228\n"
    "v_mul_f32 v10, v10, v228\n"
    "v_mul_f32 v11, v11, v228\n"
    "v_cvt_pk_bf16_f32 v0, v0, v1\n"
    "v_cvt_pk_bf16_f32 v1, v2, v3\n"
    "ds_write_b64 v234, v[0:1] offset:0\n"
    "v_mul_f32 v16, v16, v228\n"
    "v_mul_f32 v17, v17, v228\n"
    "v_mul_f32 v18, v18, v228\n"
    "v_mul_f32 v19, v19, v228\n"
    "v_cvt_pk_bf16_f32 v8, v8, v9\n"
    "v_cvt_pk_bf16_f32 v9, v10, v11\n"
    "ds_write_b64 v235, v[8:9] offset:0\n"
    "v_mul_f32 v24, v24, v228\n"
    "v_mul_f32 v25, v25, v228\n"
    "v_mul_f32 v26, v26, v228\n"
    "v_mul_f32 v27, v27, v228\n"
    "v_cvt_pk_bf16_f32 v16, v16, v17\n"
    "v_cvt_pk_bf16_f32 v17, v18, v19\n"
    "ds_write_b64 v234, v[16:17] offset:64\n"
    "v_mul_f32 v32, v32, v228\n"
    "v_mul_f32 v33, v33, v228\n"
    "v_mul_f32 v34, v34, v228\n"
    "v_mul_f32 v35, v35, v228\n"
    "v_cvt_pk_bf16_f32 v24, v24, v25\n"
    "v_cvt_pk_bf16_f32 v25, v26, v27\n"
    "ds_write_b64 v235, v[24:25] offset:64\n"
    "v_mul_f32 v40, v40, v228\n"
    "v_mul_f32 v41, v41, v228\n"
    "v_mul_f32 v42, v42, v228\n"
    "v_mul_f32 v43, v43, v228\n"
    "v_cvt_pk_bf16_f32 v32, v32, v33\n"
    "v_cvt_pk_bf16_f32 v33, v34, v35\n"
    "ds_write_b64 v234, v[32:33] offset:128\n"
    "v_mul_f32 v48, v48, v228\n"
    "v_mul_f32 v49, v49, v228\n"
    "v_mul_f32 v50, v50, v228\n"
    "v_mul_f32 v51, v51, v228\n"
    "v_cvt_pk_bf16_f32 v40, v40, v41\n"
    "v_cvt_pk_bf16_f32 v41, v42, v43\n"
    "ds_write_b64 v235, v[40:41] offset:128\n"
    "v_mul_f32 v56, v56, v228\n"
    "v_mul_f32 v57, v57, v228\n"
    "v_mul_f32 v58, v58, v228\n"
    "v_mul_f32 v59, v59, v228\n"
    "v_cvt_pk_bf16_f32 v48, v48, v49\n"
    "v_cvt_pk_bf16_f32 v49, v50, v51\n"
    "ds_write_b64 v234, v[48:49] offset:192\n"
    "v_mul_f32 v4, v4, v230\n"
    "v_mul_f32 v5, v5, v230\n"
    "v_mul_f32 v6, v6, v230\n"
    "v_mul_f32 v7, v7, v230\n"
    "v_cvt_pk_bf16_f32 v56, v56, v57\n"
    "v_cvt_pk_bf16_f32 v57, v58, v59\n"
    "ds_write_b64 v235, v[56:57] offset:192\n"
    "ds_read_b128 v[0:3], %[la] offset:0\n"
    "ds_read_b128 v[8:11], %[la] offset:1024\n"
    "ds_read_b128 v[16:19], %[la] offset:2048\n"
    "ds_read_b128 v[24:27], %[la] offset:3072\n"
    "v_mul_f32 v12, v12, v230\n"
    "v_mul_f32 v13, v13, v230\n"
    "v_mul_f32 v14, v14, v230\n"
    "v_mul_f32 v15, v15, v230\n"
    "v_cvt_pk_bf16_f32 v4, v4, v5\n"
    "v_cvt_pk_bf16_f32 v5, v6, v7\n"
    "ds_write_b64 v234, v[4:5] offset:0\n"
    "v_mul_f32 v20, v20, v230\n"
    "v_mul_f32 v21, v21, v230\n"
    "v_mul_f32 v22, v22, v230\n"
    "v_mul_f32 v23, v23, v230\n"
    "v_cvt_pk_bf16_f32 v12, v12, v13\n"
    "v_cvt_pk_bf16_f32 v13, v14, v15\n"
    "ds_write_b64 v235, v[12:13] offset:0\n"
    "v_mul_f32 v28, v28, v230\n"
    "s_add_u32 s74, s81, 0\n"
    "v_mul_f32 v29, v29, v230\n"
    "s_waitcnt lgkmcnt(5)\n"
    "v_mul_f32 v30, v30, v230\n"
    "buffer_store_dwordx4 v[0:3], %[qs], s[40:43], s74 offen sc1\n"
    "v_mul_f32 v31, v31, v230\n"
    "v_cvt_pk_bf16_f32 v20, v20, v21\n"
    "v_cvt_pk_bf16_f32 v21, v22, v23\n"
    "ds_write_b64 v234, v[20:21] offset:64\n"
    "v_mul_f32 v36, v36, v230\n"
    "s_add_u32 s74, s81, 4096\n"
    "v_mul_f32 v37, v37, v230\n"
    "s_waitcnt lgkmcnt(5)\n"
    "v_mul_f32 v38, v38, v230\n"
    "buffer_store_dwordx4 v[8:11], %[qs], s[40:43], s74 offen sc1\n"
    "v_mul_f32 v39, v39, v230\n"
    "v_cvt_pk_bf16_f32 v28, v28, v29\n"
    "v_cvt_pk_bf16_f32 v29, v30, v31\n"
    "ds_write_b64 v235, v[28:29] offset:64\n"
    "v_mul_f32 v44, v44, v230\n"
    "s_add_u32 s74, s81, 8192\n"
    "v_mul_f32 v45, v45, v230\n"
    "s_waitcnt lgkmcnt(5)\n"
    "v_mul_f32 v46, v46, v230\n"
    "buffer_store_dwordx4 v[16:19], %[qs], s[40:43], s74 offen sc1\n"
    "v_mul_f32 v47, v47, v230\n"
    "v_cvt_pk_bf16_f32 v36, v36, v37\n"
    "v_cvt_pk_bf16_f32 v37, v38, v39\n"
    "ds_write_b64 v234, v[36:37] offset:128\n"
    "v_mul_f32 v52, v52, v230\n"
    "s_add_u32 s74, s81, 12288\n"
    "v_mul_f32 v53, v53, v230\n"
    "s_waitcnt lgkmcnt(5)\n"
    "v_mul_f32 v54, v54, v230\n"
    "buffer_store_dwordx4 v[24:27], %[qs], s[40:43], s74 offen sc1\n"
    "v_mul_f32 v55, v55, v230\n"
    "v_cvt_pk_bf16_f32 v44, v44, v45\n"
    "v_cvt_pk_bf16_f32 v45, v46, v47\n"
    "ds_write_b64 v235, v[44:45] offset:128\n"
    "v_mul_f32 v60, v60, v230\n"
    "v_mul_f32 v61, v61, v230\n"
    "v_mul_f32 v62, v62, v230\n"
    "v_mul_f32 v63, v63, v230\n"
    "v_cvt_pk_bf16_f32 v52, v52, v53\n"
    "v_cvt_pk_bf16_f32 v53, v54, v55\n"
    "ds_write_b64 v234, v[52:53] offset:192\n"
    "v_cvt_pk_bf16_f32 v60, v60, v61\n"
    "v_cvt_pk_bf16_f32 v61, v62, v63\n"
    "ds_write_b64 v235, v[60:61] offset:192\n"
    "ds_read_b128 v[4:7], %[la] offset:0\n"
    "ds_read_b128 v[12:15], %[la] offset:1024\n"
    "ds_read_b128 v[20:23], %[la] offset:2048\n"
    "ds_read_b128 v[28:31], %[la] offset:3072\n"
    "v_exp_f32 v177, v177\n"
    "v_fma_f32 v181, v117, s64, v221\n"
    "v_cvt_pk_bf16_f32 v134, v188, v189\n"
    "v_exp_f32 v178, v178\n"
    "v_fma_f32 v182, v118, s64, v221\n"
    "v_exp_f32 v179, v179\n"
    "v_fma_f32 v183, v119, s64, v221\n"
    "v_cvt_pk_bf16_f32 v135, v190, v191\n"
    "v_exp_f32 v180, v180\n"
    "v_fma_f32 v184, v120, s64, v220\n"
    "v_exp_f32 v181, v181\n"
    "v_fma_f32 v185, v121, s64, v220\n"
    "v_cvt_pk_bf16_f32 v136, v176, v177\n"
    "s_add_u32 s74, s81, 16384\n"
    "v_exp_f32 v182, v182\n"
    "s_waitcnt lgkmcnt(3)\n"
    "v_fma_f32 v186, v122, s64, v220\n"
    "buffer_store_dwordx4 v[4:7], %[qs], s[40:43], s74 offen sc1\n"
    "v_exp_f32 v183, v183\n"
    "v_fma_f32 v187, v123, s64, v220\n"
    "v_cvt_pk_bf16_f32 v137, v178, v179\n"
    "s_add_u32 s74, s81, 20480\n"
    "v_exp_f32 v184, v184\n"
    "s_waitcnt lgkmcnt(2)\n"
    "v_fma_f32 v188, v124, s64, v221\n"
    "buffer_store_dwordx4 v[12:15], %[qs], s[40:43], s74 offen sc1\n"
    "v_exp_f32 v185, v185\n"
    "v_fma_f32 v189, v125, s64, v221\n"
    "v_cvt_pk_bf16_f32 v140, v180, v181\n"
    "s_add_u32 s74, s81, 24576\n"
    "v_exp_f32 v186, v186\n"
    "s_waitcnt lgkmcnt(1)\n"
    "v_fma_f32 v190, v126, s64, v221\n"
    "buffer_store_dwordx4 v[20:23], %[qs], s[40:43], s74 offen sc1\n"
    "v_exp_f32 v187, v187\n"
    "v_fma_f32 v191, v127, s64, v221\n"
    "v_cvt_pk_bf16_f32 v141, v182, v183\n"
    "s_add_u32 s74, s81, 28672\n"
    "v_exp_f32 v188, v188\n"
    "s_waitcnt lgkmcnt(0)\n"
    "v_exp_f32 v189, v189\n"
    "buffer_store_dwordx4 v[28:31], %[qs], s[40:43], s74 offen sc1\n"
    "v_cvt_pk_bf16_f32 v138, v184, v185\n"
    "v_exp_f32 v190, v190\n"
    "buffer_load_dword v232, %[qpf], s[36:39], s98 offen\n"
    "v_exp_f32 v191, v191\n"
    "v_cvt_pk_bf16_f32 v139, v186, v187\n"
    "v_cvt_pk_bf16_f32 v142, v188, v189\n"
    "v_cvt_pk_bf16_f32 v143, v190, v191\n"
    "s_cmp_eq_u32 s92, s93\n"
    "s_waitcnt vmcnt(15) lgkmcnt(0)\n"
    "s_cbranch_scc1 POST_%=\n"
    "s_barrier\n"
    "LOOPZ_%=:\n"
    "s_setprio 1\n"
    "v_mfma_f32_16x16x32_bf16 v[208:211], v[216:219], v[128:131], 0\n"
    "s_cmp_eq_u32 s92, s66\n"
    "ds_read_b64_tr_b16 v[164:165], v224 offset:8704\n"
    "s_cbranch_scc1 SEGLZ_%=\n"
    "v_mfma_f32_16x16x32_bf16 v[208:211], v[216:219], v[136:139], v[208:211]\n"
    "ds_read_b64_tr_b16 v[166:167], v224 offset:12800\n"
    "v_mfma_f32_16x16x32_bf16 v[212:215], v[216:219], v[132:135], 0\n"
    "ds_read_b64_tr_b16 v[168:169], v225 offset:512\n"
    "v_mfma_f32_16x16x32_bf16 v[212:215], v[216:219], v[140:143], v[212:215]\n"
    "ds_read_b64_tr_b16 v[170:171], v225 offset:4608\n"
    "v_mfma_f32_16x16x32_bf16 v[0:3], v[144:147], v[128:131], 0\n"
    "ds_read_b64_tr_b16 v[172:173], v225 offset:8704\n"
    "v_mfma_f32_16x16x32_bf16 v[0:3], v[148:151], v[136:139], v[0:3]\n"
    "ds_read_b64_tr_b16 v[174:175], v225 offset:12800\n"
    "v_mfma_f32_16x16x32_bf16 v[4:7], v[144:147], v[132:135], 0\n"
    "ds_read_b64_tr_b16 v[176:177], v224 offset:1024\n"
    "RSEGLZ_%=:\n"
    "v_mfma_f32_16x16x32_bf16 v[4:7], v[148:151], v[140:143], v[4:7]\n"
    "ds_read_b64_tr_b16 v[178:179], v224 offset:5120\n"
    "v_mfma_f32_16x16x32_bf16 v[8:11], v[152:155], v[128:131], 0\n"
    "ds_read_b64_tr_b16 v[180:181], v224 offset:9216\n"
    "v_mfma_f32_16x16x32_bf16 v[8:11], v[156:159], v[136:139], v[8:11]\n"
    "ds_read_b64_tr_b16 v[182:183], v224 offset:13312\n"
    "v_mfma_f32_16x16x32_bf16 v[12:15], v[152:155], v[132:135], 0\n"
    "ds_read_b64_tr_b16 v[184:185], v225 offset:1024\n"
    "v_mfma_f32_16x16x32_bf16 v[12:15], v[156:159], v[140:143], v[12:15]\n"
    "ds_read_b64_tr_b16 v[186:187], v225 offset:5120\n"
    "v_mfma_f32_16x16x32_bf16 v[16:19], v[160:163], v[128:131], 0\n"
    "ds_read_b64_tr_b16 v[188:189], v225 offset:9216\n"
    "s_waitcnt lgkmcnt(11)\n"
    "v_mfma_f32_16x16x32_bf16 v[16:19], v[164:167], v[136:139], v[16:19]\n"
    "ds_read_b64_tr_b16 v[190:191], v225 offset:13312\n"
    "v_mfma_f32_16x16x32_bf16 v[20:23], v[160:163], v[132:135], 0\n"
    "ds_read_b64_tr_b16 v[192:193], v224 offset:1536\n"
    "v_mfma_f32_16x16x32_bf16 v[20:23], v[164:167], v[140:143], v[20:23]\n"
    "ds_read_b64_tr_b16 v[194:195], v224 offset:5632\n"
    "s_waitcnt lgkmcnt(10)\n"
    "v_mfma_f32_16x16x32_bf16 v[24:27], v[168:171], v[128:131], 0\n"
    "ds_read_b64_tr_b16 v[196:197], v224 offset:9728\n"
    "v_mfma_f32_16x16x32_bf16 v[24:27], v[172:175], v[136:139], v[24:27]\n"
    "ds_read_b64_tr_b16 v[198:199], v224 offset:13824\n"
    "v_mfma_f32_16x16x32_bf16 v[28:31], v[168:171], v[132:135], 0\n"
    "ds_read_b64_tr_b16 v[200:201], v225 offset:1536\n"
    "v_mfma_f32_16x16x32_bf16 v[28:31], v[172:175], v[140:143], v[28:31]\n"
    "ds_read_b64_tr_b16 v[202:203], v225 offset:5632\n"
    "s_waitcnt lgkmcnt(10)\n"
    "v_mfma_f32_16x16x32_bf16 v[32:35], v[176:179], v[128:131], 0\n"
    "ds_read_b64_tr_b16 v[204:205], v225 offset:9728\n"
    "v_mfma_f32_16x16x32_bf16 v[32:35], v[180:183], v[136:139], v[32:35]\n"
    "ds_read_b64_tr_b16 v[206:207], v225 offset:13824\n"
    "v_mfma_f32_16x16x32_bf16 v[36:39], v[176:179], v[132:135], 0\n"
    "ds_read_b128 v[144:147], v222 offset:0\n"
    "v_mfma_f32_16x16x32_bf16 v[36:39], v[180:183], v[140:143], v[36:39]\n"
    "s_waitcnt lgkmcnt(9)\n"
    "v_mfma_f32_16x16x32_bf16 v[40:43], v[184:187], v[128:131], 0\n"
    "ds_read_b128 v[148:151], v223 offset:0\n"
    "v_mfma_f32_16x16x32_bf16 v[40:43], v[188:191], v[136:139], v[40:43]\n"
    "s_mov_b32 s85, 0\n"
    "v_mfma_f32_16x16x32_bf16 v[44:47], v[184:187], v[132:135], 0\n"
    "ds_read_b128 v[152:155], v222 offset:8192\n"
    "v_mfma_f32_16x16x32_bf16 v[44:47], v[188:191], v[140:143], v[44:47]\n"
    "s_waitcnt lgkmcnt(7)\n"
    "v_mfma_f32_16x16x32_bf16 v[48:51], v[192:195], v[128:131], 0\n"
    "ds_read_b128 v[156:159], v223 offset:8192\n"
    "v_mfma_f32_16x16x32_bf16 v[48:51], v[196:199], v[136:139], v[48:51]\n"
    "v_mfma_f32_16x16x32_bf16 v[52:55], v[192:195], v[132:135], 0\n"
    "s_cmp_lg_u32 s84, 1\n"
    "ds_read_b128 v[160:163], v222 offset:2048\n"
    "v_mfma_f32_16x16x32_bf16 v[52:55], v[196:199], v[140:143], v[52:55]\n"
    "s_waitcnt lgkmcnt(5)\n"
    "v_mfma_f32_16x16x32_bf16 v[56:59], v[200:203], v[128:131], 0\n"
    "ds_read_b128 v[164:167], v223 offset:2048\n"
    "v_mfma_f32_16x16x32_bf16 v[56:59], v[204:207], v[136:139], v[56:59]\n"
    "v_mfma_f32_16x16x32_bf16 v[60:63], v[200:203], v[132:135], 0\n"
    "ds_read_b128 v[168:171], v222 offset:10240\n"
    "v_mfma_f32_16x16x32_bf16 v[60:63], v[204:207], v[140:143], v[60:63]\n"
    "s_cbranch_scc0 QK_%=\n"
    "QKX_%=:\n"
    "s_cmp_eq_u32 s84, 0\n"
    "s_cbranch_scc1 NOQK_%=\n"
    "s_waitcnt vmcnt(5)\n"
    "ds_read_b128 v[80:83], %[sa] offset:0\n"
    "ds_read_b128 v[84:87], %[sa] offset:64\n"
    "ds_read_b128 v[88:91], %[sa] offset:128\n"
    "ds_read_b128 v[92:95], %[sa] offset:192\n"
    "s_waitcnt lgkmcnt(0)\n"
    "s_branch QK_%=\n"
    "SEGL_%=:\n"
    "v_mfma_f32_16x16x32_bf16 v[208:211], v[216:219], v[136:139], v[208:211]\n"
    "ds_read_b64_tr_b16 v[166:167], v224 offset:12800\n"
    "s_add_u32 s65, s65, 1\n"
    "v_readlane_b32 s30, %[tk], s65\n"
    "v_mfma_f32_16x16x32_bf16 v[212:215], v[216:219], v[132:135], v[212:215]\n"
    "ds_read_b64_tr_b16 v[168:169], v225 offset:512\n"
    "v_readlane_b32 s67, %[tb], s65\n"
    "v_readlane_b32 s66, %[tn], s65\n"
    "v_mfma_f32_16x16x32_bf16 v[212:215], v[216:219], v[140:143], v[212:215]\n"
    "ds_read_b64_tr_b16 v[170:171], v225 offset:4608\n"
    "s_mov_b32 s34, s30\n"
    "s_sub_u32 s66, s66, s97\n"
    "v_mfma_f32_16x16x32_bf16 v[0:3], v[144:147], v[128:131], v[0:3]\n"
    "ds_read_b64_tr_b16 v[172:173], v225 offset:8704\n"
    "s_cmp_eq_u32 s67, 1\n"
    "s_cselect_b32 s69, 0, s69\n"
    "v_mfma_f32_16x16x32_bf16 v[0:3], v[148:151], v[136:139], v[0:3]\n"
    "ds_read_b64_tr_b16 v[174:175], v225 offset:12800\n"
    "s_cselect_b32 s68, 0, s68\n"
    "s_cselect_b32 s90, 896, s90\n"
    "v_mfma_f32_16x16x32_bf16 v[4:7], v[144:147], v[132:135], v[4:7]\n"
    "ds_read_b64_tr_b16 v[176:177], v224 offset:1024\n"
    "s_cselect_b32 s70, s82, s70\n"
    "s_cselect_b32 s71, s82, s71\n"
    "s_branch RSEGL_%=\n"
    "SEGC_%=:\n"
    "v_mfma_f32_16x16x32_bf16 v[96:99], v[148:151], v[68:71], v[96:99]\n"
    "v_add_u32 v224, s86, %[vb0]\n"
    "s_cmp_eq_u32 s92, s77\n"
    "s_cselect_b32 s85, 1, 0\n"
    "s_waitcnt lgkmcnt(4)\n"
    "v_mfma_f32_16x16x32_bf16 v[96:99], v[152:155], v[72:75], v[96:99]\n"
    "ds_read_b128 v[176:179], v222 offset:4096\n"
    "s_addc_u32 s76, s76, 0\n"
    "v_mfma_f32_16x16x32_bf16 v[96:99], v[156:159], v[76:79], v[96:99]\n"
    "v_xor_b32 v225, 32, v224\n"
    "s_sub_u32 s72, s76, 1\n"
    "s_add_u32 s74, s76, 1\n"
    "v_mfma_f32_16x16x32_bf16 v[100:103], v[144:147], v[80:83], 0\n"
    "ds_read_b128 v[180:183], v223 offset:4096\n"
    "v_readlane_b32 s77, %[tn], s76\n"
    "v_readlane_b32 s79, %[td], s76\n"
    "v_mfma_f32_16x16x32_bf16 v[100:103], v[148:151], v[84:87], v[100:103]\n"
    "v_readlane_b32 s88, %[td], s74\n"
    "v_readlane_b32 s89, %[ta], s74\n"
    "v_readlane_b32 s80, %[td], s72\n"
    "v_mfma_f32_16x16x32_bf16 v[100:103], v[152:155], v[88:91], v[100:103]\n"
    "ds_read_b128 v[184:187], v222 offset:12288\n"
    "v_readlane_b32 s81, %[tq], s72\n"
    "v_readlane_b32 s42, %[te], s72\n"
    "v_mfma_f32_16x16x32_bf16 v[100:103], v[156:159], v[92:95], v[100:103]\n"
    "s_bitcmp1_b32 s80, 17\n"
    "s_cselect_b64 s[40:41], s[44:45], %[po]\n"
    "s_cselect_b32 s83, s81, 0x7f000000\n"
    "s_waitcnt lgkmcnt(5)\n"
    "v_mfma_f32_16x16x32_bf16 v[104:107], v[160:163], v[64:67], 0\n"
    "ds_read_b128 v[188:191], v223 offset:12288\n"
    "s_cselect_b32 s75, s85, 0\n"
    "v_mfma_f32_16x16x32_bf16 v[104:107], v[164:167], v[68:71], v[104:107]\n"
    "s_bitcmp1_b32 s79, 15\n"
    "s_cselect_b32 s75, s85, s75\n"
    "s_add_u32 s73, s92, 1\n"
    "s_waitcnt lgkmcnt(4)\n"
    "v_mfma_f32_16x16x32_bf16 v[104:107], v[168:171], v[72:75], v[104:107]\n"
    "ds_read_b128 v[192:195], v222 offset:6144\n"
    "s_sub_u32 s78, s77, 1\n"
    "v_mfma_f32_16x16x32_bf16 v[104:107], v[172:175], v[76:79], v[104:107]\n"
    "s_cmp_lg_u32 s88, 0\n"
    "s_cselect_b32 s84, 2, 0\n"
    "s_cselect_b32 s72, PQ_%=-BASE_%=, s72\n"
    "v_mfma_f32_16x16x32_bf16 v[108:111], v[160:163], v[80:83], 0\n"
    "ds_read_b128 v[196:199], v223 offset:6144\n"
    "s_cselect_b32 s72, s72, P_%=-BASE_%=\n"
    "s_cmp_lg_u32 s85, 0\n"
    "v_mfma_f32_16x16x32_bf16 v[108:111], v[164:167], v[84:87], v[108:111]\n"
    "s_cselect_b32 s72, G_%=-BASE_%=, s72\n"
    "s_cselect_b32 s74, 0, s79\n"
    "s_cmp_eq_u32 s73, s77\n"
    "v_mfma_f32_16x16x32_bf16 v[108:111], v[168:171], v[88:91], v[108:111]\n"
    "ds_read_b128 v[200:203], v222 offset:14336\n"
    "s_cselect_b32 s78, s77, s78\n"
    "s_cselect_b32 s84, s84, 1\n"
    "v_mfma_f32_16x16x32_bf16 v[108:111], v[172:175], v[92:95], v[108:111]\n"
    "s_cselect_b32 s72, s72, s96\n"
    "s_add_u32 s54, s56, s72\n"
    "s_addc_u32 s55, s57, 0\n"
    "s_waitcnt lgkmcnt(5)\n"
    "v_mfma_f32_16x16x32_bf16 v[112:115], v[176:179], v[64:67], 0\n"
    "ds_read_b128 v[204:207], v223 offset:14336\n"
    "s_bitcmp1_b32 s74, 16\n"
    "v_mfma_f32_16x16x32_bf16 v[112:115], v[180:183], v[68:71], v[112:115]\n"
    "s_cselect_b64 s[50:51], s[52:53], s[54:55]\n"
    "s_cmp_eq_u32 s72, G_%=-BASE_%=\n"
    "s_cselect_b32 s75, 1, s75\n"
    "s_waitcnt lgkmcnt(4)\n"
    "v_mfma_f32_16x16x32_bf16 v[112:115], v[184:187], v[72:75], v[112:115]\n"
    "s_cmp_eq_u32 s84, 0\n"
    "s_cselect_b32 s75, 1, s75\n"
    "v_mfma_f32_16x16x32_bf16 v[112:115], v[188:191], v[76:79], v[112:115]\n"
    "s_cmp_lg_u32 s75, 0\n"
    "s_cselect_b32 s93, s73, s93\n"
    "s_mov_b32 s96, F_%=-BASE_%=\n"
    "v_mfma_f32_16x16x32_bf16 v[116:119], v[176:179], v[80:83], 0\n"
    "s_mov_b32 s94, 1\n"
    "s_cmp_eq_u32 s72, PQ_%=-BASE_%=\n"
    "s_branch RSEGC_%=\n"
    "SEGQ_%=:\n"
    "v_mfma_f32_16x16x32_bf16 v[116:119], v[180:183], v[84:87], v[116:119]\n"
    "s_add_u32 m0, s82, 0\n"
    "s_add_u32 s72, s89, 0\n"
    "buffer_load_dwordx4 %[qs], s[36:39], s72 offen lds\n"
    "v_mfma_f32_16x16x32_bf16 v[116:119], v[184:187], v[88:91], v[116:119]\n"
    "ds_read_b64_tr_b16 v[144:145], v224 offset:0\n"
    "s_add_u32 m0, s82, 1024\n"
    "s_add_u32 s72, s89, 4096\n"
    "v_mfma_f32_16x16x32_bf16 v[116:119], v[188:191], v[92:95], v[116:119]\n"
    "ds_read_b64_tr_b16 v[146:147], v224 offset:4096\n"
    "buffer_load_dwordx4 %[qs], s[36:39], s72 offen lds\n"
    "s_add_u32 m0, s82, 2048\n"
    "s_waitcnt lgkmcnt(4)\n"
    "v_mfma_f32_16x16x32_bf16 v[120:123], v[192:195], v[64:67], 0\n"
    "ds_read_b64_tr_b16 v[148:149], v224 offset:8192\n"
    "s_add_u32 s72, s89, 8192\n"
    "v_mfma_f32_16x16x32_bf16 v[120:123], v[196:199], v[68:71], v[120:123]\n"
    "ds_read_b64_tr_b16 v[150:151], v224 offset:12288\n"
    "buffer_load_dwordx4 %[qs], s[36:39], s72 offen lds\n"
    "s_add_u32 m0, s82, 3072\n"
    "s_waitcnt lgkmcnt(4)\n"
    "v_mfma_f32_16x16x32_bf16 v[120:123], v[200:203], v[72:75], v[120:123]\n"
    "ds_read_b64_tr_b16 v[152:153], v225 offset:0\n"
    "s_add_u32 s72, s89, 12288\n"
    "v_mfma_f32_16x16x32_bf16 v[120:123], v[204:207], v[76:79], v[120:123]\n"
    "ds_read_b64_tr_b16 v[154:155], v225 offset:4096\n"
    "buffer_load_dwordx4 %[qs], s[36:39], s72 offen lds\n"
    "s_branch RSEGQ_%=\n"
    "SEGLZ_%=:\n"
    "v_mfma_f32_16x16x32_bf16 v[208:211], v[216:219], v[136:139], v[208:211]\n"
    "ds_read_b64_tr_b16 v[166:167], v224 offset:12800\n"
    "s_add_u32 s65, s65, 1\n"
    "v_readlane_b32 s30, %[tk], s65\n"
    "v_mfma_f32_16x16x32_bf16 v[212:215], v[216:219], v[132:135], 0\n"
    "ds_read_b64_tr_b16 v[168:169], v225 offset:512\n"
    "v_readlane_b32 s67, %[tb], s65\n"
    "v_readlane_b32 s66, %[tn], s65\n"
    "v_mfma_f32_16x16x32_bf16 v[212:215], v[216:219], v[140:143], v[212:215]\n"
    "ds_read_b64_tr_b16 v[170:171], v225 offset:4608\n"
    "s_mov_b32 s34, s30\n"
    "s_sub_u32 s66, s66, s97\n"
    "v_mfma_f32_16x16x32_bf16 v[0:3], v[144:147], v[128:131], 0\n"
    "ds_read_b64_tr_b16 v[172:173], v225 offset:8704\n"
    "s_cmp_eq_u32 s67, 1\n"
    "s_cselect_b32 s69, 0, s69\n"
    "v_mfma_f32_16x16x32_bf16 v[0:3], v[148:151], v[136:139], v[0:3]\n"
    "ds_read_b64_tr_b16 v[174:175], v225 offset:12800\n"
    "s_cselect_b32 s68, 0, s68\n"
    "s_cselect_b32 s90, 896, s90\n"
    "v_mfma_f32_16x16x32_bf16 v[4:7], v[144:147], v[132:135], 0\n"
    "ds_read_b64_tr_b16 v[176:177], v224 offset:1024\n"
    "s_cselect_b32 s70, s82, s70\n"
    "s_cselect_b32 s71, s82, s71\n"
    "s_branch RSEGLZ_%=\n"
    "G_%=:\n"
    "s_mov_b32 s75, 1\n"
    "s_cmp_lg_u32 s76, 0\n"
    "s_cbranch_scc1 EPI_%=\n"
    "G2_%=:\n"
    "s_cmp_eq_u32 s84, 1\n"
    "s_cbranch_scc1 GMAX_%=\n"
    "s_cmp_eq_u32 s84, 0\n"
    "s_cbranch_scc1 G3_%=\n"
    "s_add_u32 m0, s82, 0\n"
    "s_add_u32 s72, s89, 0\n"
    "buffer_load_dwordx4 %[qs], s[36:39], s72 offen lds\n"
    "s_add_u32 m0, s82, 1024\n"
    "s_add_u32 s72, s89, 4096\n"
    "buffer_load_dwordx4 %[qs], s[36:39], s72 offen lds\n"
    "s_add_u32 m0, s82, 2048\n"
    "s_add_u32 s72, s89, 8192\n"
    "buffer_load_dwordx4 %[qs], s[36:39], s72 offen lds\n"
    "s_add_u32 m0, s82, 3072\n"
    "s_add_u32 s72, s89, 12288\n"
    "buffer_load_dwordx4 %[qs], s[36:39], s72 offen lds\n"
    "G3_%=:\n"
    "s_bitcmp1_b32 s79, 16\n"
    "s_cbranch_scc0 GMAX_%=\n"
    "s_add_u32 s54, s56, GMAX_%=-BASE_%=\n"
    "s_addc_u32 s55, s57, 0\n"
    "s_setpc_b64 s[52:53]\n"
    "GMAX_%=:\n"
    "v_max3_f32 v192, v96, v97, v98\n"
    "v_max3_f32 v193, v100, v101, v102\n"
    "v_max3_f32 v192, v192, v99, v104\n"
    "v_max3_f32 v193, v193, v103, v108\n"
    "v_max3_f32 v192, v192, v105, v106\n"
    "v_max3_f32 v193, v193, v109, v110\n"
    "v_max3_f32 v192, v192, v107, v112\n"
    "v_max3_f32 v193, v193, v111, v116\n"
    "v_max3_f32 v192, v192, v113, v114\n"
    "v_max3_f32 v193, v193, v117, v118\n"
    "v_max3_f32 v192, v192, v115, v120\n"
    "v_max3_f32 v193, v193, v119, v124\n"
    "v_max3_f32 v192, v192, v121, v122\n"
    "v_max3_f32 v193, v193, v125, v126\n"
    "v_max_f32 v192, v192, v123\n"
    "v_max_f32 v193, v193, v127\n"
    "v_mov_b32 v194, v192\n"
    "v_mov_b32 v195, v193\n"
    "s_nop 0\n"
    "v_permlane16_swap_b32 v192, v194\n"
    "v_permlane16_swap_b32 v193, v195\n"
    "v_max_f32 v192, v192, v194\n"
    "v_max_f32 v193, v193, v195\n"
    "v_mov_b32 v194, v192\n"
    "v_mov_b32 v195, v193\n"
    "s_nop 0\n"
    "v_permlane32_swap_b32 v192, v194\n"
    "v_permlane32_swap_b32 v193, v195\n"
    "v_max_f32 v192, v192, v194\n"
    "v_max_f32 v193, v193, v195\n"
    "v_mul_f32 v192, s64, v192\n"
    "v_mul_f32 v193, s64, v193\n"
    "v_sub_f32 v192, 0xc1400000, v192\n"
    "v_sub_f32 v193, 0xc1400000, v193\n"
    "v_mov_b32 v220, v192\n"
    "v_mov_b32 v221, v193\n"
    "s_cmp_eq_u32 s84, 2\n"
    "s_cbranch_scc1 PQ_%=\n"
    "s_branch P_%=\n"
    "MASK0_%=:\n"
    "v_mov_b32 v231, 0xff800000\n"
    "s_nop 3\n"
    "v_cmp_gt_i32 s[58:59], 0, %[mt]\n"
    "v_cmp_gt_i32 s[60:61], 1, %[mt]\n"
    "v_cmp_gt_i32 s[62:63], 2, %[mt]\n"
    "v_cmp_gt_i32 vcc, 3, %[mt]\n"
    "v_cndmask_b32 v96, v96, v231, s[58:59]\n"
    "v_cmp_gt_i32 s[58:59], -4, %[mt]\n"
    "v_cndmask_b32 v97, v97, v231, s[60:61]\n"
    "v_cmp_gt_i32 s[60:61], -3, %[mt]\n"
    "v_cndmask_b32 v98, v98, v231, s[62:63]\n"
    "v_cmp_gt_i32 s[62:63], -2, %[mt]\n"
    "v_cndmask_b32 v99, v99, v231, vcc\n"
    "v_cmp_gt_i32 vcc, -1, %[mt]\n"
    "v_cndmask_b32 v100, v100, v231, s[58:59]\n"
    "v_cndmask_b32 v101, v101, v231, s[60:61]\n"
    "s_nop 0\n"
    "v_cndmask_b32 v102, v102, v231, s[62:63]\n"
    "s_nop 0\n"
    "v_cndmask_b32 v103, v103, v231, vcc\n"
    "s_nop 0\n"
    "v_mov_b32 v104, v231\n"
    "v_mov_b32 v105, v231\n"
    "v_mov_b32 v106, v231\n"
    "v_mov_b32 v107, v231\n"
    "v_mov_b32 v108, v231\n"
    "v_mov_b32 v109, v231\n"
    "v_mov_b32 v110, v231\n"
    "v_mov_b32 v111, v231\n"
    "v_mov_b32 v112, v231\n"
    "v_mov_b32 v113, v231\n"
    "v_mov_b32 v114, v231\n"
    "v_mov_b32 v115, v231\n"
    "v_mov_b32 v116, v231\n"
    "v_mov_b32 v117, v231\n"
    "v_mov_b32 v118, v231\n"
    "v_mov_b32 v119, v231\n"
    "v_mov_b32 v120, v231\n"
    "v_mov_b32 v121, v231\n"
    "v_mov_b32 v122, v231\n"
    "v_mov_b32 v123, v231\n"
    "v_mov_b32 v124, v231\n"
    "v_mov_b32 v125, v231\n"
    "v_mov_b32 v126, v231\n"
    "v_mov_b32 v127, v231\n"
    "s_setpc_b64 s[54:55]\n"
    "MASK1_%=:\n"
    "v_mov_b32 v231, 0xff800000\n"
    "s_nop 3\n"
    "v_cmp_gt_i32 s[58:59], 16, %[mt]\n"
    "v_cmp_gt_i32 s[60:61], 17, %[mt]\n"
    "v_cmp_gt_i32 s[62:63], 18, %[mt]\n"
    "v_cmp_gt_i32 vcc, 19, %[mt]\n"
    "v_cndmask_b32 v104, v104, v231, s[58:59]\n"
    "v_cmp_gt_i32 s[58:59], 12, %[mt]\n"
    "v_cndmask_b32 v105, v105, v231, s[60:61]\n"
    "v_cmp_gt_i32 s[60:61], 13, %[mt]\n"
    "v_cndmask_b32 v106, v106, v231, s[62:63]\n"
    "v_cmp_gt_i32 s[62:63], 14, %[mt]\n"
    "v_cndmask_b32 v107, v107, v231, vcc\n"
    "v_cmp_gt_i32 vcc, 15, %[mt]\n"
    "v_cndmask_b32 v108, v108, v231, s[58:59]\n"
    "v_cndmask_b32 v109, v109, v231, s[60:61]\n"
    "s_nop 0\n"
    "v_cndmask_b32 v110, v110, v231, s[62:63]\n"
    "s_nop 0\n"
    "v_cndmask_b32 v111, v111, v231, vcc\n"
    "s_nop 0\n"
    "v_mov_b32 v112, v231\n"
    "v_mov_b32 v113, v231\n"
    "v_mov_b32 v114, v231\n"
    "v_mov_b32 v115, v231\n"
    "v_mov_b32 v116, v231\n"
    "v_mov_b32 v117, v231\n"
    "v_mov_b32 v118, v231\n"
    "v_mov_b32 v119, v231\n"
    "v_mov_b32 v120, v231\n"
    "v_mov_b32 v121, v231\n"
    "v_mov_b32 v122, v231\n"
    "v_mov_b32 v123, v231\n"
    "v_mov_b32 v124, v231\n"
    "v_mov_b32 v125, v231\n"
    "v_mov_b32 v126, v231\n"
    "v_mov_b32 v127, v231\n"
    "s_setpc_b64 s[54:55]\n"
    "MASK2_%=:\n"
    "v_mov_b32 v231, 0xff800000\n"
    "s_nop 3\n"
    "v_cmp_gt_i32 s[58:59], 32, %[mt]\n"
    "v_cmp_gt_i32 s[60:61], 33, %[mt]\n"
    "v_cmp_gt_i32 s[62:63], 34, %[mt]\n"
    "v_cmp_gt_i32 vcc, 35, %[mt]\n"
    "v_cndmask_b32 v112, v112, v231, s[58:59]\n"
    "v_cmp_gt_i32 s[58:59], 28, %[mt]\n"
    "v_cndmask_b32 v113, v113, v231, s[60:61]\n"
    "v_cmp_gt_i32 s[60:61], 29, %[mt]\n"
    "v_cndmask_b32 v114, v114, v231, s[62:63]\n"
    "v_cmp_gt_i32 s[62:63], 30, %[mt]\n"
    "v_cndmask_b32 v115, v115, v231, vcc\n"
    "v_cmp_gt_i32 vcc, 31, %[mt]\n"
    "v_cndmask_b32 v116, v116, v231, s[58:59]\n"
    "v_cndmask_b32 v117, v117, v231, s[60:61]\n"
    "s_nop 0\n"
    "v_cndmask_b32 v118, v118, v231, s[62:63]\n"
    "s_nop 0\n"
    "v_cndmask_b32 v119, v119, v231, vcc\n"
    "s_nop 0\n"
    "v_mov_b32 v120, v231\n"
    "v_mov_b32 v121, v231\n"
    "v_mov_b32 v122, v231\n"
    "v_mov_b32 v123, v231\n"
    "v_mov_b32 v124, v231\n"
    "v_mov_b32 v125, v231\n"
    "v_mov_b32 v126, v231\n"
    "v_mov_b32 v127, v231\n"
    "s_setpc_b64 s[54:55]\n"
    "MASK3_%=:\n"
    "v_mov_b32 v231, 0xff800000\n"
    "s_nop 3\n"
    "v_cmp_gt_i32 s[58:59], 48, %[mt]\n"
    "v_cmp_gt_i32 s[60:61], 49, %[mt]\n"
    "v_cmp_gt_i32 s[62:63], 50, %[mt]\n"
    "v_cmp_gt_i32 vcc, 51, %[mt]\n"
    "v_cndmask_b32 v120, v120, v231, s[58:59]\n"
    "v_cmp_gt_i32 s[58:59], 44, %[mt]\n"
    "v_cndmask_b32 v121, v121, v231, s[60:61]\n"
    "v_cmp_gt_i32 s[60:61], 45, %[mt]\n"
    "v_cndmask_b32 v122, v122, v231, s[62:63]\n"
    "v_cmp_gt_i32 s[62:63], 46, %[mt]\n"
    "v_cndmask_b32 v123, v123, v231, vcc\n"
    "v_cmp_gt_i32 vcc, 47, %[mt]\n"
    "v_cndmask_b32 v124, v124, v231, s[58:59]\n"
    "v_cndmask_b32 v125, v125, v231, s[60:61]\n"
    "s_nop 0\n"
    "v_cndmask_b32 v126, v126, v231, s[62:63]\n"
    "s_nop 0\n"
    "v_cndmask_b32 v127, v127, v231, vcc\n"
    "s_nop 0\n"
    "s_setpc_b64 s[54:55]\n"
    "RARE_%=:\n"
    "v_max3_f32 v192, v96, v97, v98\n"
    "v_max3_f32 v193, v100, v101, v102\n"
    "v_max3_f32 v192, v192, v99, v104\n"
    "v_max3_f32 v193, v193, v103, v108\n"
    "v_max3_f32 v192, v192, v105, v106\n"
    "v_max3_f32 v193, v193, v109, v110\n"
    "v_max3_f32 v192, v192, v107, v112\n"
    "v_max3_f32 v193, v193, v111, v116\n"
    "v_max3_f32 v192, v192, v113, v114\n"
    "v_max3_f32 v193, v193, v117, v118\n"
    "v_max3_f32 v192, v192, v115, v120\n"
    "v_max3_f32 v193, v193, v119, v124\n"
    "v_max3_f32 v192, v192, v121, v122\n"
    "v_max3_f32 v193, v193, v125, v126\n"
    "v_max_f32 v192, v192, v123\n"
    "v_max_f32 v193, v193, v127\n"
    "v_mov_b32 v194, v192\n"
    "v_mov_b32 v195, v193\n"
    "s_nop 0\n"
    "v_permlane16_swap_b32 v192, v194\n"
    "v_permlane16_swap_b32 v193, v195\n"
    "v_max_f32 v192, v192, v194\n"
    "v_max_f32 v193, v193, v195\n"
    "v_mov_b32 v194, v192\n"
    "v_mov_b32 v195, v193\n"
    "s_nop 0\n"
    "v_permlane32_swap_b32 v192, v194\n"
    "v_permlane32_swap_b32 v193, v195\n"
    "v_max_f32 v192, v192, v194\n"
    "v_max_f32 v193, v193, v195\n"
    "v_mul_f32 v192, s64, v192\n"
    "v_mul_f32 v193, s64, v193\n"
    "v_sub_f32 v192, 0xc1400000, v192\n"
    "v_sub_f32 v193, 0xc1400000, v193\n"
    "v_min_f32 v192, v220, v192\n"
    "v_min_f32 v193, v221, v193\n"
    "v_sub_f32 v194, v192, v220\n"
    "v_sub_f32 v195, v193, v221\n"
    "v_exp_f32 v194, v194\n"
    "v_exp_f32 v195, v195\n"
    "v_mov_b32 v220, v192\n"
    "v_mov_b32 v221, v193\n"
    "v_mul_f32 v208, v208, v194\n"
    "v_mul_f32 v212, v212, v195\n"
    "v_mul_f32 v0, v0, v194\n"
    "v_mul_f32 v1, v1, v194\n"
    "v_mul_f32 v2, v2, v194\n"
    "v_mul_f32 v3, v3, v194\n"
    "v_mul_f32 v4, v4, v195\n"
    "v_mul_f32 v5, v5, v195\n"
    "v_mul_f32 v6, v6, v195\n"
    "v_mul_f32 v7, v7, v195\n"
    "v_mul_f32 v8, v8, v194\n"
    "v_mul_f32 v9, v9, v194\n"
    "v_mul_f32 v10, v10, v194\n"
    "v_mul_f32 v11, v11, v194\n"
    "v_mul_f32 v12, v12, v195\n"
    "v_mul_f32 v13, v13, v195\n"
    "v_mul_f32 v14, v14, v195\n"
    "v_mul_f32 v15, v15, v195\n"
    "v_mul_f32 v16, v16, v194\n"
    "v_mul_f32 v17, v17, v194\n"
    "v_mul_f32 v18, v18, v194\n"
    "v_mul_f32 v19, v19, v194\n"
    "v_mul_f32 v20, v20, v195\n"
    "v_mul_f32 v21, v21, v195\n"
    "v_mul_f32 v22, v22, v195\n"
    "v_mul_f32 v23, v23, v195\n"
    "v_mul_f32 v24, v24, v194\n"
    "v_mul_f32 v25, v25, v194\n"
    "v_mul_f32 v26, v26, v194\n"
    "v_mul_f32 v27, v27, v194\n"
    "v_mul_f32 v28, v28, v195\n"
    "v_mul_f32 v29, v29, v195\n"
    "v_mul_f32 v30, v30, v195\n"
    "v_mul_f32 v31, v31, v195\n"
    "v_mul_f32 v32, v32, v194\n"
    "v_mul_f32 v33, v33, v194\n"
    "v_mul_f32 v34, v34, v194\n"
    "v_mul_f32 v35, v35, v194\n"
    "v_mul_f32 v36, v36, v195\n"
    "v_mul_f32 v37, v37, v195\n"
    "v_mul_f32 v38, v38, v195\n"
    "v_mul_f32 v39, v39, v195\n"
    "v_mul_f32 v40, v40, v194\n"
    "v_mul_f32 v41, v41, v194\n"
    "v_mul_f32 v42, v42, v194\n"
    "v_mul_f32 v43, v43, v194\n"
    "v_mul_f32 v44, v44, v195\n"
    "v_mul_f32 v45, v45, v195\n"
    "v_mul_f32 v46, v46, v195\n"
    "v_mul_f32 v47, v47, v195\n"
    "v_mul_f32 v48, v48, v194\n"
    "v_mul_f32 v49, v49, v194\n"
    "v_mul_f32 v50, v50, v194\n"
    "v_mul_f32 v51, v51, v194\n"
    "v_mul_f32 v52, v52, v195\n"
    "v_mul_f32 v53, v53, v195\n"
    "v_mul_f32 v54, v54, v195\n"
    "v_mul_f32 v55, v55, v195\n"
    "v_mul_f32 v56, v56, v194\n"
    "v_mul_f32 v57, v57, v194\n"
    "v_mul_f32 v58, v58, v194\n"
    "v_mul_f32 v59, v59, v194\n"
    "v_mul_f32 v60, v60, v195\n"
    "v_mul_f32 v61, v61, v195\n"
    "v_mul_f32 v62, v62, v195\n"
    "v_mul_f32 v63, v63, v195\n"
    "v_fma_f32 v176, v96, s64, v220\n"
    "v_fma_f32 v177, v97, s64, v220\n"
    "v_fma_f32 v178, v98, s64, v220\n"
    "v_fma_f32 v179, v99, s64, v220\n"
    "v_exp_f32 v176, v176\n"
    "v_fma_f32 v180, v100, s64, v221\n"
    "v_exp_f32 v177, v177\n"
    "v_fma_f32 v181, v101, s64, v221\n"
    "v_exp_f32 v178, v178\n"
    "v_fma_f32 v182, v102, s64, v221\n"
    "v_exp_f32 v179, v179\n"
    "v_fma_f32 v183, v103, s64, v221\n"
    "v_exp_f32 v180, v180\n"
    "v_fma_f32 v184, v104, s64, v220\n"
    "v_exp_f32 v181, v181\n"
    "v_fma_f32 v185, v105, s64, v220\n"
    "v_cvt_pk_bf16_f32 v128, v176, v177\n"
    "v_exp_f32 v182, v182\n"
    "v_fma_f32 v186, v106, s64, v220\n"
    "v_exp_f32 v183, v183\n"
    "v_fma_f32 v187, v107, s64, v220\n"
    "v_cvt_pk_bf16_f32 v129, v178, v179\n"
    "v_exp_f32 v184, v184\n"
    "v_fma_f32 v188, v108, s64, v221\n"
    "v_exp_f32 v185, v185\n"
    "v_fma_f32 v189, v109, s64, v221\n"
    "v_cvt_pk_bf16_f32 v132, v180, v181\n"
    "v_exp_f32 v186, v186\n"
    "v_fma_f32 v190, v110, s64, v221\n"
    "v_exp_f32 v187, v187\n"
    "v_fma_f32 v191, v111, s64, v221\n"
    "v_cvt_pk_bf16_f32 v133, v182, v183\n"
    "v_exp_f32 v188, v188\n"
    "v_fma_f32 v176, v112, s64, v220\n"
    "v_exp_f32 v189, v189\n"
    "v_fma_f32 v177, v113, s64, v220\n"
    "v_cvt_pk_bf16_f32 v130, v184, v185\n"
    "v_exp_f32 v190, v190\n"
    "v_fma_f32 v178, v114, s64, v220\n"
    "v_exp_f32 v191, v191\n"
    "v_fma_f32 v179, v115, s64, v220\n"
    "v_cvt_pk_bf16_f32 v131, v186, v187\n"
    "v_exp_f32 v176, v176\n"
    "v_fma_f32 v180, v116, s64, v221\n"
    "v_exp_f32 v177, v177\n"
    "v_fma_f32 v181, v117, s64, v221\n"
    "v_cvt_pk_bf16_f32 v134, v188, v189\n"
    "v_exp_f32 v178, v178\n"
    "v_fma_f32 v182, v118, s64, v221\n"
    "v_exp_f32 v179, v179\n"
    "v_fma_f32 v183, v119, s64, v221\n"
    "v_cvt_pk_bf16_f32 v135, v190, v191\n"
    "v_exp_f32 v180, v180\n"
    "v_fma_f32 v184, v120, s64, v220\n"
    "v_exp_f32 v181, v181\n"
    "v_fma_f32 v185, v121, s64, v220\n"
    "v_cvt_pk_bf16_f32 v136, v176, v177\n"
    "v_exp_f32 v182, v182\n"
    "v_fma_f32 v186, v122, s64, v220\n"
    "v_exp_f32 v183, v183\n"
    "v_fma_f32 v187, v123, s64, v220\n"
    "v_cvt_pk_bf16_f32 v137, v178, v179\n"
    "v_exp_f32 v184, v184\n"
    "v_fma_f32 v188, v124, s64, v221\n"
    "v_exp_f32 v185, v185\n"
    "v_fma_f32 v189, v125, s64, v221\n"
    "v_cvt_pk_bf16_f32 v140, v180, v181\n"
    "v_exp_f32 v186, v186\n"
    "v_fma_f32 v190, v126, s64, v221\n"
    "v_exp_f32 v187, v187\n"
    "v_fma_f32 v191, v127, s64, v221\n"
    "v_cvt_pk_bf16_f32 v141, v182, v183\n"
    "v_exp_f32 v188, v188\n"
    "v_exp_f32 v189, v189\n"
    "v_cvt_pk_bf16_f32 v138, v184, v185\n"
    "v_exp_f32 v190, v190\n"
    "v_exp_f32 v191, v191\n"
    "v_cvt_pk_bf16_f32 v139, v186, v187\n"
    "v_cvt_pk_bf16_f32 v142, v188, v189\n"
    "v_cvt_pk_bf16_f32 v143, v190, v191\n"
    "s_cmp_eq_u32 s92, s93\n"
    "s_branch TAIL_%=\n"
    "POST_%=:\n"
    "s_lshl_b32 s72, s84, 2\n"
    "s_or_b32 s72, s72, s97\n"
    "s_cmp_eq_u32 s72, 3\n"
    "s_cbranch_scc1 POST1_%=\n"
    "s_barrier\n"
    "POST1_%=:\n"
    "s_cmp_lg_u32 s92, 0\n"
    "s_cbranch_scc1 POST2_%=\n"
    "ds_read_b64_tr_b16 v[144:145], v224 offset:0\n"
    "ds_read_b64_tr_b16 v[146:147], v224 offset:4096\n"
    "ds_read_b64_tr_b16 v[148:149], v224 offset:8192\n"
    "ds_read_b64_tr_b16 v[150:151], v224 offset:12288\n"
    "ds_read_b64_tr_b16 v[152:153], v225 offset:0\n"
    "ds_read_b64_tr_b16 v[154:155], v225 offset:4096\n"
    "ds_read_b64_tr_b16 v[156:157], v225 offset:8192\n"
    "ds_read_b64_tr_b16 v[158:159], v225 offset:12288\n"
    "ds_read_b64_tr_b16 v[160:161], v224 offset:512\n"
    "ds_read_b64_tr_b16 v[162:163], v224 offset:4608\n"
    "POST2_%=:\n"
    "s_mov_b32 s93, 0x7fffffff\n"
    "s_cmp_ge_i32 s87, 0\n"
    "s_cbranch_scc1 FPX_%=\n"
    "FPOK_%=:\n"
    "s_lshr_b32 s72, s80, 17\n"
    "s_sub_u32 s73, s76, 1\n"
    "s_add_u32 s74, s92, 1\n"
    "s_and_b32 s72, s72, s85\n"
    "s_cselect_b32 s87, s73, -1\n"
    "s_cselect_b32 s93, s74, s93\n"
    "s_bitcmp1_b32 s79, 18\n"
    "s_cbranch_scc1 PINX_%=\n"
    "s_cmp_eq_u32 s84, 0\n"
    "s_cbranch_scc1 LASTM_%=\n"
    "PINOK_%=:\n"
    "s_bitcmp1_b32 s79, 15\n"
    "s_cbranch_scc1 IDLE_%=\n"
    "RESUME_%=:\n"
    "s_waitcnt lgkmcnt(0)\n"
    "s_cmp_lg_u32 s85, 0\n"
    "s_cbranch_scc1 LOOPZ_%=\n"
    "s_branch LOOP_%=\n"
    "IDLE_%=:\n"
    "s_cmp_ge_i32 s92, s78\n"
    "s_cbranch_scc1 RESUME_%=\n"
    "s_mov_b32 s85, 0\n"
    "v_readlane_b32 s98, v233, s92\n"
    "s_cmp_lg_u32 s92, s66\n"
    "s_cbranch_scc1 PAi_%=\n"
    "s_add_u32 s65, s65, 1\n"
    "v_readlane_b32 s30, %[tk], s65\n"
    "v_readlane_b32 s67, %[tb], s65\n"
    "v_readlane_b32 s66, %[tn], s65\n"
    "s_mov_b32 s34, s30\n"
    "s_sub_u32 s66, s66, s97\n"
    "s_cmp_eq_u32 s67, 1\n"
    "s_cselect_b32 s69, 0, s69\n"
    "s_cselect_b32 s68, 0, s68\n"
    "s_cselect_b32 s90, 896, s90\n"
    "s_cselect_b32 s70, s82, s70\n"
    "s_cselect_b32 s71, s82, s71\n"
    "PAi_%=:\n"
    "s_barrier\n"
    "s_add_u32 m0, s68, s70\n"
    "s_nop 0\n"
    "buffer_load_dwordx4 %[ksrc], s[28:31], s67 offen lds\n"
    "s_add_u32 m0, m0, s90\n"
    "s_nop 0\n"
    "buffer_load_dwordx4 %[ksrc], s[28:31], s67 offen offset:128 lds\n"
    "s_add_u32 m0, s68, s71\n"
    "s_add_u32 s68, s68, 32768\n"
    "buffer_load_dwordx4 %[vsrc], s[32:35], s67 offen lds\n"
    "s_add_u32 m0, m0, 896\n"
    "s_and_b32 s68, s68, s69\n"
    "buffer_load_dwordx4 %[vsrc], s[32:35], s67 offen offset:128 lds\n"
    "s_add_u32 s67, s67, 65536\n"
    "buffer_load_dword v232, %[qpf], s[36:39], s98 offen\n"
    "s_add_u32 s86, s86, 32768\n"
    "s_and_b32 s86, s86, 0x1ffff\n"
    "s_add_u32 s92, s92, 1\n"
    "v_add_u32 v222, s86, %[kb0]\n"
    "v_xor_b32 v223, 64, v222\n"
    "s_mov_b32 s94, 0\n"
    "s_cmp_eq_u32 s92, s93\n"
    "s_waitcnt vmcnt(6) lgkmcnt(0)\n"
    "s_cbranch_scc1 POST_%=\n"
    "s_barrier\n"
    "s_branch IDLE_%=\n"
    "FPX_%=:\n"
    "v_readlane_b32 s95, v226, s87\n"
    "v_readlane_b32 s91, %[td], s87\n"
    "s_xor_b32 s48, s95, 0x9e3779b9\n"
    "s_mul_i32 s49, s95, 0x85ebca6b\n"
    "s_add_u32 s49, s49, 0xc2b2ae35\n"
    "s_lshr_b32 s91, s91, 19\n"
    "s_mul_i32 s91, s91, 327680\n"
    "s_add_u32 s72, s91, 8192\n"
    "v_mov_b32 v230, s48\n"
    "v_mov_b32 v231, s49\n"
    "s_mov_b64 exec, 1\n"
    "buffer_store_dwordx2 v[230:231], %[woff], s[44:47], s72 offen sc1\n"
    "s_mov_b64 exec, -1\n"
    "s_branch FPOK_%=\n"
    "LASTM_%=:\n"
    "v_readlane_b32 s80, %[td], s76\n"
    "v_readlane_b32 s81, %[tq], s76\n"
    "v_readlane_b32 s42, %[te], s76\n"
    "s_bitcmp1_b32 s80, 17\n"
    "s_cselect_b64 s[40:41], s[44:45], %[po]\n"
    "s_cselect_b32 s83, s81, 0x7f000000\n"
    "s_cmp_lg_u32 s85, 0\n"
    "s_cbranch_scc1 RESUME_%=\n"
    "v_mfma_f32_16x16x32_bf16 v[208:211], v[216:219], v[128:131], v[208:211]\n"
    "ds_read_b64_tr_b16 v[164:165], v224 offset:8704\n"
    "ds_read_b64_tr_b16 v[166:167], v224 offset:12800\n"
    "v_mfma_f32_16x16x32_bf16 v[208:211], v[216:219], v[136:139], v[208:211]\n"
    "ds_read_b64_tr_b16 v[168:169], v225 offset:512\n"
    "ds_read_b64_tr_b16 v[170:171], v225 offset:4608\n"
    "v_mfma_f32_16x16x32_bf16 v[212:215], v[216:219], v[132:135], v[212:215]\n"
    "ds_read_b64_tr_b16 v[172:173], v225 offset:8704\n"
    "ds_read_b64_tr_b16 v[174:175], v225 offset:12800\n"
    "v_mfma_f32_16x16x32_bf16 v[212:215], v[216:219], v[140:143], v[212:215]\n"
    "ds_read_b64_tr_b16 v[176:177], v224 offset:1024\n"
    "ds_read_b64_tr_b16 v[178:179], v224 offset:5120\n"
    "v_mfma_f32_16x16x32_bf16 v[0:3], v[144:147], v[128:131], v[0:3]\n"
    "ds_read_b64_tr_b16 v[180:181], v224 offset:9216\n"
    "ds_read_b64_tr_b16 v[182:183], v224 offset:13312\n"
    "v_mfma_f32_16x16x32_bf16 v[0:3], v[148:151], v[136:139], v[0:3]\n"
    "v_mfma_f32_16x16x32_bf16 v[8:11], v[152:155], v[128:131], v[8:11]\n"
    "v_mfma_f32_16x16x32_bf16 v[8:11], v[156:159], v[136:139], v[8:11]\n"
    "ds_read_b64_tr_b16 v[184:185], v225 offset:1024\n"
    "ds_read_b64_tr_b16 v[186:187], v225 offset:5120\n"
    "v_rcp_f32 v228, v208\n"
    "v_rcp_f32 v230, v212\n"
    "v_mov_b32 v209, v212\n"
    "v_mfma_f32_16x16x32_bf16 v[16:19], v[160:163], v[128:131], v[16:19]\n"
    "ds_read_b64_tr_b16 v[188:189], v225 offset:9216\n"
    "ds_read_b64_tr_b16 v[190:191], v225 offset:13312\n"
    "v_mov_b32 v210, v220\n"
    "v_mov_b32 v211, v221\n"
    "buffer_store_dwordx4 v[208:211], %[woff], s[44:47], s83 offen sc1\n"
    "s_waitcnt lgkmcnt(12)\n"
    "v_mfma_f32_16x16x32_bf16 v[16:19], v[164:167], v[136:139], v[16:19]\n"
    "ds_read_b64_tr_b16 v[192:193], v224 offset:1536\n"
    "ds_read_b64_tr_b16 v[194:195], v224 offset:5632\n"
    "v_mul_f32 v0, v0, v228\n"
    "v_mul_f32 v1, v1, v228\n"
    "v_mul_f32 v2, v2, v228\n"
    "s_waitcnt lgkmcnt(10)\n"
    "v_mfma_f32_16x16x32_bf16 v[24:27], v[168:171], v[128:131], v[24:27]\n"
    "ds_read_b64_tr_b16 v[196:197], v224 offset:9728\n"
    "ds_read_b64_tr_b16 v[198:199], v224 offset:13824\n"
    "v_mul_f32 v3, v3, v228\n"
    "v_mul_f32 v8, v8, v228\n"
    "v_mul_f32 v9, v9, v228\n"
    "v_mfma_f32_16x16x32_bf16 v[24:27], v[172:175], v[136:139], v[24:27]\n"
    "ds_read_b64_tr_b16 v[200:201], v225 offset:1536\n"
    "ds_read_b64_tr_b16 v[202:203], v225 offset:5632\n"
    "v_mul_f32 v10, v10, v228\n"
    "v_mul_f32 v11, v11, v228\n"
    "v_cvt_pk_bf16_f32 v0, v0, v1\n"
    "s_waitcnt lgkmcnt(10)\n"
    "v_mfma_f32_16x16x32_bf16 v[32:35], v[176:179], v[128:131], v[32:35]\n"
    "ds_read_b64_tr_b16 v[204:205], v225 offset:9728\n"
    "ds_read_b64_tr_b16 v[206:207], v225 offset:13824\n"
    "v_cvt_pk_bf16_f32 v1, v2, v3\n"
    "ds_write_b64 v234, v[0:1] offset:0\n"
    "v_mul_f32 v16, v16, v228\n"
    "v_mfma_f32_16x16x32_bf16 v[32:35], v[180:183], v[136:139], v[32:35]\n"
    "v_mul_f32 v17, v17, v228\n"
    "v_mul_f32 v18, v18, v228\n"
    "v_mul_f32 v19, v19, v228\n"
    "v_cvt_pk_bf16_f32 v8, v8, v9\n"
    "v_cvt_pk_bf16_f32 v9, v10, v11\n"
    "s_waitcnt lgkmcnt(9)\n"
    "v_mfma_f32_16x16x32_bf16 v[40:43], v[184:187], v[128:131], v[40:43]\n"
    "ds_write_b64 v235, v[8:9] offset:0\n"
    "v_mul_f32 v24, v24, v228\n"
    "v_mul_f32 v25, v25, v228\n"
    "v_mul_f32 v26, v26, v228\n"
    "v_mul_f32 v27, v27, v228\n"
    "v_mfma_f32_16x16x32_bf16 v[40:43], v[188:191], v[136:139], v[40:43]\n"
    "v_cvt_pk_bf16_f32 v16, v16, v17\n"
    "v_cvt_pk_bf16_f32 v17, v18, v19\n"
    "ds_write_b64 v234, v[16:17] offset:64\n"
    "v_mul_f32 v32, v32, v228\n"
    "v_mul_f32 v33, v33, v228\n"
    "s_waitcnt lgkmcnt(7)\n"
    "v_mfma_f32_16x16x32_bf16 v[48:51], v[192:195], v[128:131], v[48:51]\n"
    "v_mul_f32 v34, v34, v228\n"
    "v_mul_f32 v35, v35, v228\n"
    "v_cvt_pk_bf16_f32 v24, v24, v25\n"
    "v_cvt_pk_bf16_f32 v25, v26, v27\n"
    "ds_write_b64 v235, v[24:25] offset:64\n"
    "v_mfma_f32_16x16x32_bf16 v[48:51], v[196:199], v[136:139], v[48:51]\n"
    "v_mul_f32 v40, v40, v228\n"
    "v_mul_f32 v41, v41, v228\n"
    "v_mul_f32 v42, v42, v228\n"
    "v_mul_f32 v43, v43, v228\n"
    "v_cvt_pk_bf16_f32 v32, v32, v33\n"
    "s_waitcnt lgkmcnt(4)\n"
    "v_mfma_f32_16x16x32_bf16 v[56:59], v[200:203], v[128:131], v[56:59]\n"
    "v_cvt_pk_bf16_f32 v33, v34, v35\n"
    "ds_write_b64 v234, v[32:33] offset:128\n"
    "v_mul_f32 v48, v48, v228\n"
    "v_mul_f32 v49, v49, v228\n"
    "v_mul_f32 v50, v50, v228\n"
    "v_mfma_f32_16x16x32_bf16 v[56:59], v[204:207], v[136:139], v[56:59]\n"
    "v_mul_f32 v51, v51, v228\n"
    "v_cvt_pk_bf16_f32 v40, v40, v41\n"
    "v_cvt_pk_bf16_f32 v41, v42, v43\n"
    "ds_write_b64 v235, v[40:41] offset:128\n"
    "v_mfma_f32_16x16x32_bf16 v[4:7], v[144:147], v[132:135], v[4:7]\n"
    "v_mfma_f32_16x16x32_bf16 v[4:7], v[148:151], v[140:143], v[4:7]\n"
    "v_mfma_f32_16x16x32_bf16 v[12:15], v[152:155], v[132:135], v[12:15]\n"
    "v_mfma_f32_16x16x32_bf16 v[12:15], v[156:159], v[140:143], v[12:15]\n"
    "v_mul_f32 v56, v56, v228\n"
    "v_mul_f32 v57, v57, v228\n"
    "v_mul_f32 v58, v58, v228\n"
    "v_mul_f32 v59, v59, v228\n"
    "v_cvt_pk_bf16_f32 v48, v48, v49\n"
    "v_mfma_f32_16x16x32_bf16 v[20:23], v[160:163], v[132:135], v[20:23]\n"
    "v_cvt_pk_bf16_f32 v49, v50, v51\n"
    "ds_write_b64 v234, v[48:49] offset:192\n"
    "v_mul_f32 v4, v4, v230\n"
    "v_mul_f32 v5, v5, v230\n"
    "v_mul_f32 v6, v6, v230\n"
    "v_mfma_f32_16x16x32_bf16 v[20:23], v[164:167], v[140:143], v[20:23]\n"
    "v_mul_f32 v7, v7, v230\n"
    "v_cvt_pk_bf16_f32 v56, v56, v57\n"
    "v_cvt_pk_bf16_f32 v57, v58, v59\n"
    "ds_write_b64 v235, v[56:57] offset:192\n"
    "ds_read_b128 v[0:3], %[la] offset:0\n"
    "v_mfma_f32_16x16x32_bf16 v[28:31], v[168:171], v[132:135], v[28:31]\n"
    "ds_read_b128 v[8:11], %[la] offset:1024\n"
    "ds_read_b128 v[16:19], %[la] offset:2048\n"
    "ds_read_b128 v[24:27], %[la] offset:3072\n"
    "v_mul_f32 v12, v12, v230\n"
    "v_mul_f32 v13, v13, v230\n"
    "v_mfma_f32_16x16x32_bf16 v[28:31], v[172:175], v[140:143], v[28:31]\n"
    "v_mul_f32 v14, v14, v230\n"
    "v_mul_f32 v15, v15, v230\n"
    "v_cvt_pk_bf16_f32 v4, v4, v5\n"
    "v_cvt_pk_bf16_f32 v5, v6, v7\n"
    "ds_write_b64 v234, v[4:5] offset:0\n"
    "v_mfma_f32_16x16x32_bf16 v[36:39], v[176:179], v[132:135], v[36:39]\n"
    "v_mul_f32 v20, v20, v230\n"
    "v_mul_f32 v21, v21, v230\n"
    "v_mul_f32 v22, v22, v230\n"
    "v_mul_f32 v23, v23, v230\n"
    "v_cvt_pk_bf16_f32 v12, v12, v13\n"
    "v_mfma_f32_16x16x32_bf16 v[36:39], v[180:183], v[140:143], v[36:39]\n"
    "v_cvt_pk_bf16_f32 v13, v14, v15\n"
    "ds_write_b64 v235, v[12:13] offset:0\n"
    "v_mul_f32 v28, v28, v230\n"
    "v_mul_f32 v29, v29, v230\n"
    "v_mul_f32 v30, v30, v230\n"
    "v_mfma_f32_16x16x32_bf16 v[44:47], v[184:187], v[132:135], v[44:47]\n"
    "v_mul_f32 v31, v31, v230\n"
    "v_cvt_pk_bf16_f32 v20, v20, v21\n"
    "v_cvt_pk_bf16_f32 v21, v22, v23\n"
    "ds_write_b64 v234, v[20:21] offset:64\n"
    "s_add_u32 s74, s81, 0\n"
    "v_mfma_f32_16x16x32_bf16 v[44:47], v[188:191], v[140:143], v[44:47]\n"
    "s_waitcnt lgkmcnt(6)\n"
    "buffer_store_dwordx4 v[0:3], %[qs], s[40:43], s74 offen sc1\n"
    "v_mul_f32 v36, v36, v230\n"
    "v_mul_f32 v37, v37, v230\n"
    "v_mul_f32 v38, v38, v230\n"
    "v_mfma_f32_16x16x32_bf16 v[52:55], v[192:195], v[132:135], v[52:55]\n"
    "v_mul_f32 v39, v39, v230\n"
    "v_cvt_pk_bf16_f32 v28, v28, v29\n"
    "v_cvt_pk_bf16_f32 v29, v30, v31\n"
    "ds_write_b64 v235, v[28:29] offset:64\n"
    "s_add_u32 s74, s81, 4096\n"
    "v_mfma_f32_16x16x32_bf16 v[52:55], v[196:199], v[140:143], v[52:55]\n"
    "s_waitcnt lgkmcnt(6)\n"
    "buffer_store_dwordx4 v[8:11], %[qs], s[40:43], s74 offen sc1\n"
    "v_mul_f32 v44, v44, v230\n"
    "v_mul_f32 v45, v45, v230\n"
    "v_mul_f32 v46, v46, v230\n"
    "v_mfma_f32_16x16x32_bf16 v[60:63], v[200:203], v[132:135], v[60:63]\n"
    "v_mul_f32 v47, v47, v230\n"
    "v_cvt_pk_bf16_f32 v36, v36, v37\n"
    "v_cvt_pk_bf16_f32 v37, v38, v39\n"
    "ds_write_b64 v234, v[36:37] offset:128\n"
    "s_add_u32 s74, s81, 8192\n"
    "v_mfma_f32_16x16x32_bf16 v[60:63], v[204:207], v[140:143], v[60:63]\n"
    "s_waitcnt lgkmcnt(6)\n"
    "buffer_store_dwordx4 v[16:19], %[qs], s[40:43], s74 offen sc1\n"
    "v_mul_f32 v52, v52, v230\n"
    "v_mul_f32 v53, v53, v230\n"
    "v_mul_f32 v54, v54, v230\n"
    "v_mul_f32 v55, v55, v230\n"
    "v_cvt_pk_bf16_f32 v44, v44, v45\n"
    "v_cvt_pk_bf16_f32 v45, v46, v47\n"
    "ds_write_b64 v235, v[44:45] offset:128\n"
    "s_add_u32 s74, s81, 12288\n"
    "s_waitcnt lgkmcnt(6)\n"
    "buffer_store_dwordx4 v[24:27], %[qs], s[40:43], s74 offen sc1\n"
    "v_mul_f32 v60, v60, v230\n"
    "v_mul_f32 v61, v61, v230\n"
    "v_mul_f32 v62, v62, v230\n"
    "v_mul_f32 v63, v63, v230\n"
    "v_cvt_pk_bf16_f32 v52, v52, v53\n"
    "v_cvt_pk_bf16_f32 v53, v54, v55\n"
    "ds_write_b64 v234, v[52:53] offset:192\n"
    "v_cvt_pk_bf16_f32 v60, v60, v61\n"
    "v_cvt_pk_bf16_f32 v61, v62, v63\n"
    "ds_write_b64 v235, v[60:61] offset:192\n"
    "ds_read_b128 v[4:7], %[la] offset:0\n"
    "ds_read_b128 v[12:15], %[la] offset:1024\n"
    "ds_read_b128 v[20:23], %[la] offset:2048\n"
    "ds_read_b128 v[28:31], %[la] offset:3072\n"
    "s_add_u32 s74, s81, 16384\n"
    "s_waitcnt lgkmcnt(3)\n"
    "buffer_store_dwordx4 v[4:7], %[qs], s[40:43], s74 offen sc1\n"
    "s_add_u32 s74, s81, 20480\n"
    "s_waitcnt lgkmcnt(2)\n"
    "buffer_store_dwordx4 v[12:15], %[qs], s[40:43], s74 offen sc1\n"
    "s_add_u32 s74, s81, 24576\n"
    "s_waitcnt lgkmcnt(1)\n"
    "buffer_store_dwordx4 v[20:23], %[qs], s[40:43], s74 offen sc1\n"
    "s_add_u32 s74, s81, 28672\n"
    "s_waitcnt lgkmcnt(0)\n"
    "buffer_store_dwordx4 v[28:31], %[qs], s[40:43], s74 offen sc1\n"
    "s_branch FINAL_%=\n"
    "PINX_%=:\n"
    "s_sub_u32 s72, s77, 4\n"
    "s_max_i32 s93, s74, s72\n"
    "s_sub_u32 s74, s77, s92\n"
    "s_cmp_eq_u32 s74, 4\n"
    "s_cbranch_scc1 PIN4_%=\n"
    "s_cmp_gt_u32 s74, 2\n"
    "s_cbranch_scc1 PINOK_%=\n"
    "s_sub_u32 s73, s82, 131072\n"
    "s_cmp_eq_u32 s74, 0\n"
    "s_cbranch_scc1 PIN0_%=\n"
    "s_xor_b32 s72, s86, 65536\n"
    "s_add_u32 s73, s73, s72\n"
    "s_add_u32 s95, s95, 1\n"
    "s_cmp_eq_u32 s74, 1\n"
    "s_cbranch_scc1 PIN1_%=\n"
    "s_add_u32 s72, s91, 8192\n"
    "s_mov_b32 s74, 0x8000\n"
    "s_mov_b64 exec, 1\n"
    "POLL_%=:\n"
    "v_readfirstlane_b32 s58, v226\n"
    "v_readfirstlane_b32 s59, v227\n"
    "s_sub_u32 s74, s74, 1\n"
    "s_cbranch_scc1 POLLX_%=\n"
    "s_cmp_eq_u64 s[58:59], s[48:49]\n"
    "s_cbranch_scc1 POLLX_%=\n"
    "s_sleep 4\n"
    "buffer_load_dwordx2 v[226:227], %[woff], s[44:47], s72 offen sc1\n"
    "s_waitcnt vmcnt(0)\n"
    "s_branch POLL_%=\n"
    "POLLX_%=:\n"
    "v_mov_b32 v230, s95\n"
    "buffer_store_dword v230, %[woff], s[44:47], s72 offen offset:128 sc1\n"
    "s_mov_b64 exec, -1\n"
    "s_add_u32 m0, s82, 2048\n"
    "s_nop 0\n"
    "buffer_load_dwordx4 %[woff], s[44:47], s91 offen sc1 lds\n"
    "PIN1_%=:\n"
    "s_add_u32 m0, s73, 0\n"
    "s_nop 0\n"
    "buffer_load_dwordx4 %[qs], s[44:47], s91 offen sc1 lds\n"
    "s_add_u32 s91, s91, 4096\n"
    "s_add_u32 m0, s73, 1024\n"
    "s_nop 0\n"
    "buffer_load_dwordx4 %[qs], s[44:47], s91 offen sc1 lds\n"
    "s_add_u32 s91, s91, 4096\n"
    "s_add_u32 m0, s73, 2048\n"
    "s_nop 0\n"
    "buffer_load_dwordx4 %[qs], s[44:47], s91 offen sc1 lds\n"
    "s_add_u32 s91, s91, 4096\n"
    "s_add_u32 m0, s73, 3072\n"
    "s_nop 0\n"
    "buffer_load_dwordx4 %[qs], s[44:47], s91 offen sc1 lds\n"
    "s_add_u32 s91, s91, 4096\n"
    "s_branch PINOK_%=\n"
    "PIN4_%=:\n"
    "v_readlane_b32 s95, v226, s76\n"
    "v_readlane_b32 s91, %[td], s76\n"
    "s_xor_b32 s48, s95, 0x9e3779b9\n"
    "s_mul_i32 s49, s95, 0x85ebca6b\n"
    "s_add_u32 s49, s49, 0xc2b2ae35\n"
    "s_lshr_b32 s91, s91, 19\n"
    "s_mul_i32 s91, s91, 327680\n"
    "s_add_u32 s72, s91, 8192\n"
    "s_add_u32 s93, s93, 1\n"
    "s_mov_b64 exec, 1\n"
    "buffer_load_dwordx2 v[226:227], %[woff], s[44:47], s72 offen sc1\n"
    "s_mov_b64 exec, -1\n"
    "s_branch PINOK_%=\n"
    "PIN0_%=:\n"
    "s_waitcnt vmcnt(5)\n"
    "s_add_u32 s72, s86, 32768\n"
    "s_and_b32 s72, s72, 0x1ffff\n"
    "s_sub_u32 s72, s72, 131072\n"
    "s_sub_u32 s73, s86, 131072\n"
    "v_add_u32 v68, s73, v234\n"
    "v_add_u32 v70, s72, v234\n"
    "v_add_u32 v69, s73, v235\n"
    "v_add_u32 v71, s72, v235\n"
    "ds_read_b128 v[64:67], %[la] offset:2048\n"
    "ds_read_b64 v[96:97], v68 offset:0\n"
    "ds_read_b64 v[98:99], v69 offset:0\n"
    "ds_read_b64 v[100:101], v68 offset:64\n"
    "ds_read_b64 v[102:103], v69 offset:64\n"
    "ds_read_b64 v[104:105], v68 offset:128\n"
    "ds_read_b64 v[106:107], v69 offset:128\n"
    "ds_read_b64 v[108:109], v68 offset:192\n"
    "ds_read_b64 v[110:111], v69 offset:192\n"
    "s_waitcnt lgkmcnt(4)\n"
    "ds_read_b64 v[112:113], v70 offset:0\n"
    "ds_read_b64 v[114:115], v71 offset:0\n"
    "ds_read_b64 v[116:117], v70 offset:64\n"
    "ds_read_b64 v[118:119], v71 offset:64\n"
    "ds_read_b64 v[120:121], v70 offset:128\n"
    "ds_read_b64 v[122:123], v71 offset:128\n"
    "ds_read_b64 v[124:125], v70 offset:192\n"
    "ds_read_b64 v[126:127], v71 offset:192\n"
    "s_waitcnt lgkmcnt(4)\n"
    "v_readlane_b32 s80, %[td], s76\n"
    "v_readlane_b32 s81, %[tq], s76\n"
    "v_readlane_b32 s42, %[te], s76\n"
    "s_bitcmp1_b32 s80, 17\n"
    "s_cselect_b64 s[40:41], s[44:45], %[po]\n"
    "s_cselect_b32 s83, s81, 0x7f000000\n"
    "s_branch LOOP_%=\n"
    "NOQK_%=:\n"
    "s_setprio 0\n"
    "s_waitcnt lgkmcnt(0)\n"
    "s_mov_b32 s75, 0\n"
    "s_bitcmp1_b32 s80, 18\n"
    "s_cbranch_scc1 EPI_PIN_%=\n"
    "EPI_%=:\n"
    "v_rcp_f32 v228, v208\n"
    "v_rcp_f32 v230, v212\n"
    "v_mov_b32 v209, v212\n"
    "v_mov_b32 v210, v220\n"
    "v_mov_b32 v211, v221\n"
    "buffer_store_dwordx4 v[208:211], %[woff], s[44:47], s83 offen sc1\n"
    "v_mul_f32 v0, v0, v228\n"
    "v_mul_f32 v1, v1, v228\n"
    "v_mul_f32 v2, v2, v228\n"
    "v_mul_f32 v3, v3, v228\n"
    "v_mul_f32 v8, v8, v228\n"
    "v_mul_f32 v9, v9, v228\n"
    "v_mul_f32 v10, v10, v228\n"
    "v_mul_f32 v11, v11, v228\n"
    "v_cvt_pk_bf16_f32 v0, v0, v1\n"
    "v_cvt_pk_bf16_f32 v1, v2, v3\n"
    "ds_write_b64 v234, v[0:1] offset:0\n"
    "v_mul_f32 v16, v16, v228\n"
    "v_mul_f32 v17, v17, v228\n"
    "v_mul_f32 v18, v18, v228\n"
    "v_mul_f32 v19, v19, v228\n"
    "v_cvt_pk_bf16_f32 v8, v8, v9\n"
    "v_cvt_pk_bf16_f32 v9, v10, v11\n"
    "ds_write_b64 v235, v[8:9] offset:0\n"
    "v_mul_f32 v24, v24, v228\n"
    "v_mul_f32 v25, v25, v228\n"
    "v_mul_f32 v26, v26, v228\n"
    "v_mul_f32 v27, v27, v228\n"
    "v_cvt_pk_bf16_f32 v16, v16, v17\n"
    "v_cvt_pk_bf16_f32 v17, v18, v19\n"
    "ds_write_b64 v234, v[16:17] offset:64\n"
    "v_mul_f32 v32, v32, v228\n"
    "v_mul_f32 v33, v33, v228\n"
    "v_mul_f32 v34, v34, v228\n"
    "v_mul_f32 v35, v35, v228\n"
    "v_cvt_pk_bf16_f32 v24, v24, v25\n"
    "v_cvt_pk_bf16_f32 v25, v26, v27\n"
    "ds_write_b64 v235, v[24:25] offset:64\n"
    "v_mul_f32 v40, v40, v228\n"
    "v_mul_f32 v41, v41, v228\n"
    "v_mul_f32 v42, v42, v228\n"
    "v_mul_f32 v43, v43, v228\n"
    "v_cvt_pk_bf16_f32 v32, v32, v33\n"
    "v_cvt_pk_bf16_f32 v33, v34, v35\n"
    "ds_write_b64 v234, v[32:33] offset:128\n"
    "v_mul_f32 v48, v48, v228\n"
    "v_mul_f32 v49, v49, v228\n"
    "v_mul_f32 v50, v50, v228\n"
    "v_mul_f32 v51, v51, v228\n"
    "v_cvt_pk_bf16_f32 v40, v40, v41\n"
    "v_cvt_pk_bf16_f32 v41, v42, v43\n"
    "ds_write_b64 v235, v[40:41] offset:128\n"
    "v_mul_f32 v56, v56, v228\n"
    "v_mul_f32 v57, v57, v228\n"
    "v_mul_f32 v58, v58, v228\n"
    "v_mul_f32 v59, v59, v228\n"
    "v_cvt_pk_bf16_f32 v48, v48, v49\n"
    "v_cvt_pk_bf16_f32 v49, v50, v51\n"
    "ds_write_b64 v234, v[48:49] offset:192\n"
    "v_mul_f32 v4, v4, v230\n"
    "v_mul_f32 v5, v5, v230\n"
    "v_mul_f32 v6, v6, v230\n"
    "v_mul_f32 v7, v7, v230\n"
    "v_cvt_pk_bf16_f32 v56, v56, v57\n"
    "v_cvt_pk_bf16_f32 v57, v58, v59\n"
    "ds_write_b64 v235, v[56:57] offset:192\n"
    "ds_read_b128 v[0:3], %[la] offset:0\n"
    "ds_read_b128 v[8:11], %[la] offset:1024\n"
    "ds_read_b128 v[16:19], %[la] offset:2048\n"
    "ds_read_b128 v[24:27], %[la] offset:3072\n"
    "v_mul_f32 v12, v12, v230\n"
    "v_mul_f32 v13, v13, v230\n"
    "v_mul_f32 v14, v14, v230\n"
    "v_mul_f32 v15, v15, v230\n"
    "v_cvt_pk_bf16_f32 v4, v4, v5\n"
    "v_cvt_pk_bf16_f32 v5, v6, v7\n"
    "ds_write_b64 v234, v[4:5] offset:0\n"
    "v_mul_f32 v20, v20, v230\n"
    "v_mul_f32 v21, v21, v230\n"
    "v_mul_f32 v22, v22, v230\n"
    "v_mul_f32 v23, v23, v230\n"
    "v_cvt_pk_bf16_f32 v12, v12, v13\n"
    "v_cvt_pk_bf16_f32 v13, v14, v15\n"
    "ds_write_b64 v235, v[12:13] offset:0\n"
    "v_mul_f32 v28, v28, v230\n"
    "s_add_u32 s74, s81, 0\n"
    "v_mul_f32 v29, v29, v230\n"
    "s_waitcnt lgkmcnt(5)\n"
    "v_mul_f32 v30, v30, v230\n"
    "buffer_store_dwordx4 v[0:3], %[qs], s[40:43], s74 offen sc1\n"
    "v_mul_f32 v31, v31, v230\n"
    "v_cvt_pk_bf16_f32 v20, v20, v21\n"
    "v_cvt_pk_bf16_f32 v21, v22, v23\n"
    "ds_write_b64 v234, v[20:21] offset:64\n"
    "v_mul_f32 v36, v36, v230\n"
    "v_mul_f32 v37, v37, v230\n"
    "v_mul_f32 v38, v38, v230\n"
    "s_add_u32 s74, s81, 4096\n"
    "v_mul_f32 v39, v39, v230\n"
    "s_waitcnt lgkmcnt(5)\n"
    "v_cvt_pk_bf16_f32 v28, v28, v29\n"
    "buffer_store_dwordx4 v[8:11], %[qs], s[40:43], s74 offen sc1\n"
    "v_cvt_pk_bf16_f32 v29, v30, v31\n"
    "ds_write_b64 v235, v[28:29] offset:64\n"
    "v_mul_f32 v44, v44, v230\n"
    "v_mul_f32 v45, v45, v230\n"
    "v_mul_f32 v46, v46, v230\n"
    "v_mul_f32 v47, v47, v230\n"
    "v_cvt_pk_bf16_f32 v36, v36, v37\n"
    "s_add_u32 s74, s81, 8192\n"
    "v_cvt_pk_bf16_f32 v37, v38, v39\n"
    "s_waitcnt lgkmcnt(5)\n"
    "ds_write_b64 v234, v[36:37] offset:128\n"
    "buffer_store_dwordx4 v[16:19], %[qs], s[40:43], s74 offen sc1\n"
    "v_mul_f32 v52, v52, v230\n"
    "v_mul_f32 v53, v53, v230\n"
    "v_mul_f32 v54, v54, v230\n"
    "v_mul_f32 v55, v55, v230\n"
    "v_cvt_pk_bf16_f32 v44, v44, v45\n"
    "v_cvt_pk_bf16_f32 v45, v46, v47\n"
    "ds_write_b64 v235, v[44:45] offset:128\n"
    "s_add_u32 s74, s81, 12288\n"
    "v_mul_f32 v60, v60, v230\n"
    "s_waitcnt lgkmcnt(6)\n"
    "v_mul_f32 v61, v61, v230\n"
    "buffer_store_dwordx4 v[24:27], %[qs], s[40:43], s74 offen sc1\n"
    "v_mul_f32 v62, v62, v230\n"
    "v_mul_f32 v63, v63, v230\n"
    "v_cvt_pk_bf16_f32 v52, v52, v53\n"
    "v_cvt_pk_bf16_f32 v53, v54, v55\n"
    "ds_write_b64 v234, v[52:53] offset:192\n"
    "v_cvt_pk_bf16_f32 v60, v60, v61\n"
    "v_cvt_pk_bf16_f32 v61, v62, v63\n"
    "ds_write_b64 v235, v[60:61] offset:192\n"
    "ds_read_b128 v[4:7], %[la] offset:0\n"
    "ds_read_b128 v[12:15], %[la] offset:1024\n"
    "ds_read_b128 v[20:23], %[la] offset:2048\n"
    "ds_read_b128 v[28:31], %[la] offset:3072\n"
    "s_add_u32 s74, s81, 16384\n"
    "s_waitcnt lgkmcnt(3)\n"
    "buffer_store_dwordx4 v[4:7], %[qs], s[40:43], s74 offen sc1\n"
    "s_add_u32 s74, s81, 20480\n"
    "s_waitcnt lgkmcnt(2)\n"
    "buffer_store_dwordx4 v[12:15], %[qs], s[40:43], s74 offen sc1\n"
    "s_add_u32 s74, s81, 24576\n"
    "s_waitcnt lgkmcnt(1)\n"
    "buffer_store_dwordx4 v[20:23], %[qs], s[40:43], s74 offen sc1\n"
    "s_add_u32 s74, s81, 28672\n"
    "s_waitcnt lgkmcnt(0)\n"
    "buffer_store_dwordx4 v[28:31], %[qs], s[40:43], s74 offen sc1\n"
    "s_cmp_lg_u32 s75, 0\n"
    "s_cbranch_scc1 G2_%=\n"
    "s_branch FINAL_%=\n"
    "EPI_PIN_%=:\n"
    "v_min_f32 v192, v220, v66\n"
    "v_min_f32 v193, v221, v67\n"
    "v_sub_f32 v228, v192, v220\n"
    "v_sub_f32 v230, v193, v221\n"
    "v_sub_f32 v229, v192, v66\n"
    "v_sub_f32 v231, v193, v67\n"
    "v_exp_f32 v229, v229\n"
    "v_exp_f32 v231, v231\n"
    "v_exp_f32 v228, v228\n"
    "v_exp_f32 v230, v230\n"
    "v_mul_f32 v229, v229, v64\n"
    "v_mul_f32 v231, v231, v65\n"
    "v_fma_f32 v192, v208, v228, v229\n"
    "v_fma_f32 v193, v212, v230, v231\n"
    "v_rcp_f32 v192, v192\n"
    "v_rcp_f32 v193, v193\n"
    "s_nop 0\n"
    "v_mul_f32 v228, v228, v192\n"
    "v_mul_f32 v230, v230, v193\n"
    "v_mul_f32 v229, v229, v192\n"
    "v_mul_f32 v231, v231, v193\n"
    "s_waitcnt vmcnt(0)\n"
    "v_mul_f32 v0, v0, v228\n"
    "v_mul_f32 v1, v1, v228\n"
    "v_mul_f32 v2, v2, v228\n"
    "v_mul_f32 v3, v3, v228\n"
    "v_lshlrev_b32 v192, 16, v96\n"
    "v_and_b32 v193, 0xffff0000, v96\n"
    "v_lshlrev_b32 v194, 16, v97\n"
    "v_and_b32 v195, 0xffff0000, v97\n"
    "v_fmac_f32 v0, v192, v229\n"
    "v_fmac_f32 v1, v193, v229\n"
    "v_fmac_f32 v2, v194, v229\n"
    "v_fmac_f32 v3, v195, v229\n"
    "v_mul_f32 v8, v8, v228\n"
    "v_mul_f32 v9, v9, v228\n"
    "v_mul_f32 v10, v10, v228\n"
    "v_mul_f32 v11, v11, v228\n"
    "v_lshlrev_b32 v196, 16, v98\n"
    "v_and_b32 v197, 0xffff0000, v98\n"
    "v_lshlrev_b32 v198, 16, v99\n"
    "v_and_b32 v199, 0xffff0000, v99\n"
    "v_fmac_f32 v8, v196, v229\n"
    "v_fmac_f32 v9, v197, v229\n"
    "v_fmac_f32 v10, v198, v229\n"
    "v_fmac_f32 v11, v199, v229\n"
    "v_cvt_pk_bf16_f32 v0, v0, v1\n"
    "v_cvt_pk_bf16_f32 v1, v2, v3\n"
    "ds_write_b64 v234, v[0:1] offset:0\n"
    "v_mul_f32 v16, v16, v228\n"
    "v_mul_f32 v17, v17, v228\n"
    "v_mul_f32 v18, v18, v228\n"
    "v_mul_f32 v19, v19, v228\n"
    "v_lshlrev_b32 v192, 16, v100\n"
    "v_and_b32 v193, 0xffff0000, v100\n"
    "v_lshlrev_b32 v194, 16, v101\n"
    "v_and_b32 v195, 0xffff0000, v101\n"
    "v_fmac_f32 v16, v192, v229\n"
    "v_fmac_f32 v17, v193, v229\n"
    "v_fmac_f32 v18, v194, v229\n"
    "v_fmac_f32 v19, v195, v229\n"
    "v_cvt_pk_bf16_f32 v8, v8, v9\n"
    "v_cvt_pk_bf16_f32 v9, v10, v11\n"
    "ds_write_b64 v235, v[8:9] offset:0\n"
    "v_mul_f32 v24, v24, v228\n"
    "v_mul_f32 v25, v25, v228\n"
    "v_mul_f32 v26, v26, v228\n"
    "v_mul_f32 v27, v27, v228\n"
    "v_lshlrev_b32 v196, 16, v102\n"
    "v_and_b32 v197, 0xffff0000, v102\n"
    "v_lshlrev_b32 v198, 16, v103\n"
    "v_and_b32 v199, 0xffff0000, v103\n"
    "v_fmac_f32 v24, v196, v229\n"
    "v_fmac_f32 v25, v197, v229\n"
    "v_fmac_f32 v26, v198, v229\n"
    "v_fmac_f32 v27, v199, v229\n"
    "v_cvt_pk_bf16_f32 v16, v16, v17\n"
    "v_cvt_pk_bf16_f32 v17, v18, v19\n"
    "ds_write_b64 v234, v[16:17] offset:64\n"
    "v_mul_f32 v32, v32, v228\n"
    "v_mul_f32 v33, v33, v228\n"
    "v_mul_f32 v34, v34, v228\n"
    "v_mul_f32 v35, v35, v228\n"
    "v_lshlrev_b32 v192, 16, v104\n"
    "v_and_b32 v193, 0xffff0000, v104\n"
    "v_lshlrev_b32 v194, 16, v105\n"
    "v_and_b32 v195, 0xffff0000, v105\n"
    "v_fmac_f32 v32, v192, v229\n"
    "v_fmac_f32 v33, v193, v229\n"
    "v_fmac_f32 v34, v194, v229\n"
    "v_fmac_f32 v35, v195, v229\n"
    "v_cvt_pk_bf16_f32 v24, v24, v25\n"
    "v_cvt_pk_bf16_f32 v25, v26, v27\n"
    "ds_write_b64 v235, v[24:25] offset:64\n"
    "v_mul_f32 v40, v40, v228\n"
    "v_mul_f32 v41, v41, v228\n"
    "v_mul_f32 v42, v42, v228\n"
    "v_mul_f32 v43, v43, v228\n"
    "v_lshlrev_b32 v196, 16, v106\n"
    "v_and_b32 v197, 0xffff0000, v106\n"
    "v_lshlrev_b32 v198, 16, v107\n"
    "v_and_b32 v199, 0xffff0000, v107\n"
    "v_fmac_f32 v40, v196, v229\n"
    "v_fmac_f32 v41, v197, v229\n"
    "v_fmac_f32 v42, v198, v229\n"
    "v_fmac_f32 v43, v199, v229\n"
    "v_cvt_pk_bf16_f32 v32, v32, v33\n"
    "v_cvt_pk_bf16_f32 v33, v34, v35\n"
    "ds_write_b64 v234, v[32:33] offset:128\n"
    "v_mul_f32 v48, v48, v228\n"
    "v_mul_f32 v49, v49, v228\n"
    "v_mul_f32 v50, v50, v228\n"
    "v_mul_f32 v51, v51, v228\n"
    "v_lshlrev_b32 v192, 16, v108\n"
    "v_and_b32 v193, 0xffff0000, v108\n"
    "v_lshlrev_b32 v194, 16, v109\n"
    "v_and_b32 v195, 0xffff0000, v109\n"
    "v_fmac_f32 v48, v192, v229\n"
    "v_fmac_f32 v49, v193, v229\n"
    "v_fmac_f32 v50, v194, v229\n"
    "v_fmac_f32 v51, v195, v229\n"
    "v_cvt_pk_bf16_f32 v40, v40, v41\n"
    "v_cvt_pk_bf16_f32 v41, v42, v43\n"
    "ds_write_b64 v235, v[40:41] offset:128\n"
    "v_mul_f32 v56, v56, v228\n"
    "v_mul_f32 v57, v57, v228\n"
    "v_mul_f32 v58, v58, v228\n"
    "v_mul_f32 v59, v59, v228\n"
    "v_lshlrev_b32 v196, 16, v110\n"
    "v_and_b32 v197, 0xffff0000, v110\n"
    "v_lshlrev_b32 v198, 16, v111\n"
    "v_and_b32 v199, 0xffff0000, v111\n"
    "v_fmac_f32 v56, v196, v229\n"
    "v_fmac_f32 v57, v197, v229\n"
    "v_fmac_f32 v58, v198, v229\n"
    "v_fmac_f32 v59, v199, v229\n"
    "v_cvt_pk_bf16_f32 v48, v48, v49\n"
    "v_cvt_pk_bf16_f32 v49, v50, v51\n"
    "ds_write_b64 v234, v[48:49] offset:192\n"
    "v_mul_f32 v4, v4, v230\n"
    "v_mul_f32 v5, v5, v230\n"
    "v_mul_f32 v6, v6, v230\n"
    "v_mul_f32 v7, v7, v230\n"
    "v_lshlrev_b32 v192, 16, v112\n"
    "v_and_b32 v193, 0xffff0000, v112\n"
    "v_lshlrev_b32 v194, 16, v113\n"
    "v_and_b32 v195, 0xffff0000, v113\n"
    "v_fmac_f32 v4, v192, v231\n"
    "v_fmac_f32 v5, v193, v231\n"
    "v_fmac_f32 v6, v194, v231\n"
    "v_fmac_f32 v7, v195, v231\n"
    "v_cvt_pk_bf16_f32 v56, v56, v57\n"
    "v_cvt_pk_bf16_f32 v57, v58, v59\n"
    "ds_write_b64 v235, v[56:57] offset:192\n"
    "ds_read_b128 v[0:3], %[la] offset:0\n"
    "ds_read_b128 v[8:11], %[la] offset:1024\n"
    "ds_read_b128 v[16:19], %[la] offset:2048\n"
    "ds_read_b128 v[24:27], %[la] offset:3072\n"
    "v_mul_f32 v12, v12, v230\n"
    "v_mul_f32 v13, v13, v230\n"
    "v_mul_f32 v14, v14, v230\n"
    "v_mul_f32 v15, v15, v230\n"
    "v_lshlrev_b32 v196, 16, v114\n"
    "v_and_b32 v197, 0xffff0000, v114\n"
    "v_lshlrev_b32 v198, 16, v115\n"
    "v_and_b32 v199, 0xffff0000, v115\n"
    "v_fmac_f32 v12, v196, v231\n"
    "v_fmac_f32 v13, v197, v231\n"
    "v_fmac_f32 v14, v198, v231\n"
    "v_fmac_f32 v15, v199, v231\n"
    "v_cvt_pk_bf16_f32 v4, v4, v5\n"
    "v_cvt_pk_bf16_f32 v5, v6, v7\n"
    "ds_write_b64 v234, v[4:5] offset:0\n"
    "s_add_u32 s74, s81, 0\n"
    "v_mul_f32 v20, v20, v230\n"
    "s_waitcnt lgkmcnt(4)\n"
    "v_mul_f32 v21, v21, v230\n"
    "buffer_store_dwordx4 v[0:3], %[qs], s[40:43], s74 offen sc1\n"
    "v_mul_f32 v22, v22, v230\n"
    "v_mul_f32 v23, v23, v230\n"
    "v_lshlrev_b32 v192, 16, v116\n"
    "v_and_b32 v193, 0xffff0000, v116\n"
    "v_lshlrev_b32 v194, 16, v117\n"
    "v_and_b32 v195, 0xffff0000, v117\n"
    "v_fmac_f32 v20, v192, v231\n"
    "v_fmac_f32 v21, v193, v231\n"
    "v_fmac_f32 v22, v194, v231\n"
    "v_fmac_f32 v23, v195, v231\n"
    "v_cvt_pk_bf16_f32 v12, v12, v13\n"
    "v_cvt_pk_bf16_f32 v13, v14, v15\n"
    "ds_write_b64 v235, v[12:13] offset:0\n"
    "v_mul_f32 v28, v28, v230\n"
    "v_mul_f32 v29, v29, v230\n"
    "v_mul_f32 v30, v30, v230\n"
    "v_mul_f32 v31, v31, v230\n"
    "v_lshlrev_b32 v196, 16, v118\n"
    "v_and_b32 v197, 0xffff0000, v118\n"
    "v_lshlrev_b32 v198, 16, v119\n"
    "v_and_b32 v199, 0xffff0000, v119\n"
    "s_add_u32 s74, s81, 4096\n"
    "v_fmac_f32 v28, v196, v231\n"
    "s_waitcnt lgkmcnt(4)\n"
    "v_fmac_f32 v29, v197, v231\n"
    "buffer_store_dwordx4 v[8:11], %[qs], s[40:43], s74 offen sc1\n"
    "v_fmac_f32 v30, v198, v231\n"
    "v_fmac_f32 v31, v199, v231\n"
    "v_cvt_pk_bf16_f32 v20, v20, v21\n"
    "v_cvt_pk_bf16_f32 v21, v22, v23\n"
    "ds_write_b64 v234, v[20:21] offset:64\n"
    "v_mul_f32 v36, v36, v230\n"
    "v_mul_f32 v37, v37, v230\n"
    "v_mul_f32 v38, v38, v230\n"
    "v_mul_f32 v39, v39, v230\n"
    "v_lshlrev_b32 v192, 16, v120\n"
    "v_and_b32 v193, 0xffff0000, v120\n"
    "v_lshlrev_b32 v194, 16, v121\n"
    "v_and_b32 v195, 0xffff0000, v121\n"
    "v_fmac_f32 v36, v192, v231\n"
    "v_fmac_f32 v37, v193, v231\n"
    "v_fmac_f32 v38, v194, v231\n"
    "v_fmac_f32 v39, v195, v231\n"
    "v_cvt_pk_bf16_f32 v28, v28, v29\n"
    "v_cvt_pk_bf16_f32 v29, v30, v31\n"
    "ds_write_b64 v235, v[28:29] offset:64\n"
    "v_mul_f32 v44, v44, v230\n"
    "s_add_u32 s74, s81, 8192\n"
    "v_mul_f32 v45, v45, v230\n"
    "s_waitcnt lgkmcnt(5)\n"
    "v_mul_f32 v46, v46, v230\n"
    "buffer_store_dwordx4 v[16:19], %[qs], s[40:43], s74 offen sc1\n"
    "v_mul_f32 v47, v47, v230\n"
    "v_lshlrev_b32 v196, 16, v122\n"
    "v_and_b32 v197, 0xffff0000, v122\n"
    "v_lshlrev_b32 v198, 16, v123\n"
    "v_and_b32 v199, 0xffff0000, v123\n"
    "v_fmac_f32 v44, v196, v231\n"
    "v_fmac_f32 v45, v197, v231\n"
    "v_fmac_f32 v46, v198, v231\n"
    "v_fmac_f32 v47, v199, v231\n"
    "v_cvt_pk_bf16_f32 v36, v36, v37\n"
    "v_cvt_pk_bf16_f32 v37, v38, v39\n"
    "ds_write_b64 v234, v[36:37] offset:128\n"
    "v_mul_f32 v52, v52, v230\n"
    "v_mul_f32 v53, v53, v230\n"
    "v_mul_f32 v54, v54, v230\n"
    "v_mul_f32 v55, v55, v230\n"
    "v_lshlrev_b32 v192, 16, v124\n"
    "v_and_b32 v193, 0xffff0000, v124\n"
    "v_lshlrev_b32 v194, 16, v125\n"
    "v_and_b32 v195, 0xffff0000, v125\n"
    "v_fmac_f32 v52, v192, v231\n"
    "s_add_u32 s74, s81, 12288\n"
    "v_fmac_f32 v53, v193, v231\n"
    "s_waitcnt lgkmcnt(5)\n"
    "v_fmac_f32 v54, v194, v231\n"
    "buffer_store_dwordx4 v[24:27], %[qs], s[40:43], s74 offen sc1\n"
    "v_fmac_f32 v55, v195, v231\n"
    "v_cvt_pk_bf16_f32 v44, v44, v45\n"
    "v_cvt_pk_bf16_f32 v45, v46, v47\n"
    "ds_write_b64 v235, v[44:45] offset:128\n"
    "v_mul_f32 v60, v60, v230\n"
    "v_mul_f32 v61, v61, v230\n"
    "v_mul_f32 v62, v62, v230\n"
    "v_mul_f32 v63, v63, v230\n"
    "v_lshlrev_b32 v196, 16, v126\n"
    "v_and_b32 v197, 0xffff0000, v126\n"
    "v_lshlrev_b32 v198, 16, v127\n"
    "v_and_b32 v199, 0xffff0000, v127\n"
    "v_fmac_f32 v60, v196, v231\n"
    "v_fmac_f32 v61, v197, v231\n"
    "v_fmac_f32 v62, v198, v231\n"
    "v_fmac_f32 v63, v199, v231\n"
    "v_cvt_pk_bf16_f32 v52, v52, v53\n"
    "v_cvt_pk_bf16_f32 v53, v54, v55\n"
    "ds_write_b64 v234, v[52:53] offset:192\n"
    "v_cvt_pk_bf16_f32 v60, v60, v61\n"
    "v_cvt_pk_bf16_f32 v61, v62, v63\n"
    "ds_write_b64 v235, v[60:61] offset:192\n"
    "ds_read_b128 v[4:7], %[la] offset:0\n"
    "ds_read_b128 v[12:15], %[la] offset:1024\n"
    "ds_read_b128 v[20:23], %[la] offset:2048\n"
    "ds_read_b128 v[28:31], %[la] offset:3072\n"
    "s_add_u32 s74, s81, 16384\n"
    "s_waitcnt lgkmcnt(3)\n"
    "buffer_store_dwordx4 v[4:7], %[qs], s[40:43], s74 offen sc1\n"
    "s_add_u32 s74, s81, 20480\n"
    "s_waitcnt lgkmcnt(2)\n"
    "buffer_store_dwordx4 v[12:15], %[qs], s[40:43], s74 offen sc1\n"
    "s_add_u32 s74, s81, 24576\n"
    "s_waitcnt lgkmcnt(1)\n"
    "buffer_store_dwordx4 v[20:23], %[qs], s[40:43], s74 offen sc1\n"
    "s_add_u32 s74, s81, 28672\n"
    "s_waitcnt lgkmcnt(0)\n"
    "buffer_store_dwordx4 v[28:31], %[qs], s[40:43], s74 offen sc1\n"
    "FINAL_%=:\n"
    "s_waitcnt vmcnt(0)\n"
    "s_cmp_lt_i32 s87, 0\n"
    "s_cbranch_scc1 FIN1_%=\n"
    "v_readlane_b32 s95, v226, s87\n"
    "v_readlane_b32 s91, %[td], s87\n"
    "s_xor_b32 s48, s95, 0x9e3779b9\n"
    "s_mul_i32 s49, s95, 0x85ebca6b\n"
    "s_add_u32 s49, s49, 0xc2b2ae35\n"
    "s_lshr_b32 s91, s91, 19\n"
    "s_mul_i32 s91, s91, 327680\n"
    "s_add_u32 s72, s91, 8192\n"
    "v_mov_b32 v230, s48\n"
    "v_mov_b32 v231, s49\n"
    "s_mov_b64 exec, 1\n"
    "buffer_store_dwordx2 v[230:231], %[woff], s[44:47], s72 offen sc1\n"
    "s_mov_b64 exec, -1\n"
    "FIN1_%=:\n"
    "s_bitcmp1_b32 s80, 17\n"
    "s_cbranch_scc0 FIN2_%=\n"
    "s_nop 1\n"
    "v_readlane_b32 s95, v226, s76\n"
    "v_readlane_b32 s91, %[td], s76\n"
    "s_xor_b32 s48, s95, 0x9e3779b9\n"
    "s_mul_i32 s49, s95, 0x85ebca6b\n"
    "s_add_u32 s49, s49, 0xc2b2ae35\n"
    "s_lshr_b32 s91, s91, 19\n"
    "s_mul_i32 s91, s91, 327680\n"
    "s_add_u32 s72, s91, 8192\n"
    "v_mov_b32 v230, s48\n"
    "v_mov_b32 v231, s49\n"
    "s_mov_b64 exec, 1\n"
    "buffer_store_dwordx2 v[230:231], %[woff], s[44:47], s72 offen sc1\n"
    "s_mov_b64 exec, -1\n"
    "FIN2_%=:\n"
    "s_waitcnt vmcnt(0)\n"
    ".Lattn_end:\n"
      :
      : [pk] "s"(pk), [pv] "s"(pv), [pq] "s"(pq), [po] "s"(po), [pw] "s"(pw), [qbytes] "s"(qbytes), [wbytes] "s"(wbytes),
        [wave] "s"(w), [lds] "s"(ldsb),
        [kb0] "v"(kb0), [vb0] "v"(vb0), [ksrc] "v"(ksrc), [vsrc] "v"(vsrc), [qs] "v"(qs),
        [la] "v"(la), [sa] "v"(sa), [qpf] "v"(qpf), [mt] "v"(mt), [woff] "v"(woff), [ta] "v"(ta), [tb] "v"(tb), [tk] "v"(tk), [tn] "v"(tn), [te] "v"(te), [tq] "v"(tq),
        [td] "v"(td), [epa] "v"(epa)
      : "v0", "v1", "v2", "v3", "v4", "v5", "v6", "v7", "v8", "v9", "v10", "v11", "v12", "v13", "v14", "v15", "v16", "v17", "v18", "v19", "v20", "v21", "v22", "v23", "v24", "v25", "v26", "v27", "v28", "v29", "v30", "v31", "v32", "v33", "v34", "v35", "v36", "v37", "v38", "v39", "v40", "v41", "v42", "v43", "v44", "v45", "v46", "v47", "v48", "v49", "v50", "v51", "v52", "v53", "v54", "v55", "v56", "v57", "v58", "v59", "v60", "v61", "v62", "v63", "v64", "v65", "v66", "v67", "v68", "v69", "v70", "v71", "v72", "v73", "v74", "v75", "v76", "v77", "v78", "v79", "v80", "v81", "v82", "v83", "v84", "v85", "v86", "v87", "v88", "v89", "v90", "v91", "v92", "v93", "v94", "v95", "v96", "v97", "v98", "v99", "v100", "v101", "v102", "v103", "v104", "v105", "v106", "v107", "v108", "v109", "v110", "v111", "v112", "v113", "v114", "v115", "v116", "v117", "v118", "v119", "v120", "v121", "v122", "v123", "v124", "v125", "v126", "v127", "v128", "v129", "v130", "v131", "v132", "v133", "v134", "v135", "v136", "v137", "v138", "v139", "v140", "v141", "v142", "v143", "v144", "v145", "v146", "v147", "v148", "v149", "v150", "v151", "v152", "v153", "v154", "v155", "v156", "v157", "v158", "v159", "v160", "v161", "v162", "v163", "v164", "v165", "v166", "v167", "v168", "v169", "v170", "v171", "v172", "v173", "v174", "v175", "v176", "v177", "v178", "v179", "v180", "v181", "v182", "v183", "v184", "v185", "v186", "v187", "v188", "v189", "v190", "v191", "v192", "v193", "v194", "v195", "v196", "v197", "v198", "v199", "v200", "v201", "v202", "v203", "v204", "v205", "v206", "v207", "v208", "v209", "v210", "v211", "v212", "v213", "v214", "v215", "v216", "v217", "v218", "v219", "v220", "v221", "v222", "v223", "v224", "v225", "v226", "v227", "v228", "v229", "v230", "v231", "v232", "v233", "v234", "v235",
      "s28", "s29", "s30", "s31", "s32", "s33", "s34", "s35", "s36", "s37", "s38", "s39", "s40", "s41", "s42", "s43", "s44", "s45", "s46", "s47", "s48", "s49", "s50", "s51", "s52", "s53", "s54", "s55", "s56", "s57", "s58", "s59", "s60", "s61", "s62", "s63", "s64", "s65", "s66", "s67", "s68", "s69", "s70", "s71", "s72", "s73", "s74", "s75", "s76", "s77", "s78", "s79", "s80", "s81", "s82", "s83", "s84", "s85", "s86", "s87", "s88", "s89", "s90", "s91", "s92", "s93", "s94", "s95", "s96", "s97", "s98", "vcc", "scc", "memory");
    }
  }
}

extern "C" void kernel_launch(void* const* d_in, const int* in_sizes, int n_in, void* d_out, int out_size, void* d_ws,
                              size_t ws_size, hipStream_t stream) {
  const int T = in_sizes[0] / (16 * 128);
  const int nseq = in_sizes[3] - 1;
  if (T <= 0 || nseq <= 0) return;
  const unsigned wbytes = (ws_size > 0x40000000u ? 0x40000000u : (unsigned)ws_size) - 4096u;
  const int items_max = T / 64 + nseq;
  const int passes = (items_max + NBIN * 62 - 1) / (NBIN * 62);
  for (int chunk = 0; chunk < passes; ++chunk)
    varlen_attn_kernel<<<256, 512, 0, stream>>>((const char*)d_in[0], (const char*)d_in[1], (const char*)d_in[2],
                                                (const int*)d_in[3], (char*)d_out, (char*)d_ws, nseq, T, wbytes, chunk);
}
```
